```python
import torch

def _softmax(x, axis=-1):
    return torch.softmax(x, dim=axis)
def _gelu(x):
    return torch.nn.functional.gelu(x, approximate="tanh")
def _silu(x):
    return torch.nn.functional.silu(x)
def _sigmoid(x):
    return torch.sigmoid(x)
def _layernorm(x, axis=-1, eps=1e-5):
    m = x.mean(dim=axis, keepdim=True)
    v = x.var(dim=axis, keepdim=True, unbiased=False)
    return (x - m) / torch.sqrt(v + eps)
def _scatter_add(tgt, idx, src, axis=0):
    return tgt.clone().index_add_(axis, idx, src)
def _segment_sum(src, seg, n):
    out = torch.zeros((n,) + src.shape[1:], dtype=src.dtype)
    return out.index_add_(0, seg, src)
def _pad(x, pw):
    flat = tuple(v for pair in reversed(pw) for v in pair)
    return torch.nn.functional.pad(x, flat)

kernel_name = 'concat_heads__sliding_block_cached__compressed_kv_cla__decode'

def setup_inputs():

    _w = (lambda a: (lambda t: t.to(torch.bfloat16) if t.is_floating_point() else t)(torch.as_tensor(a)))
    def _A_setup():
        B, Sq, D = 2, 1, 512
        Hq, Hkv, Sk, Dh = 32, 8, 8192, 128
        assert Hq % Hkv == 0, (Hq, Hkv)
        return {
            "x": _w((lambda *s: torch.randn(*s, dtype=torch.bfloat16) * 1.6)(B, Sq, D)),
            "k_cache": _w((lambda *s: torch.randn(*s, dtype=torch.bfloat16) * 1.6)(B, Hkv, Sk, Dh)),
            "v_cache": _w((lambda *s: torch.randn(*s, dtype=torch.bfloat16) * 0.05)(B, Hkv, Sk, Dh)),
            "wq": _w((lambda *s: torch.randn(*s, dtype=torch.bfloat16) * 1.6)(D, Hq * Dh) * (D ** -0.5)),
            "wo": _w((lambda *s: torch.randn(*s, dtype=torch.bfloat16) * 0.05)(Hq * Dh, D) * ((Hq * Dh) ** -0.5)),
        }
    _A_in = _A_setup()

    def _B_setup():
        B, H, Sq, Sk, D, M = 2, 32, 1, 8192, 128, 512
        Dc = 4 * D
        return {
            "x": _w((lambda *s: torch.randn(*s, dtype=torch.bfloat16) * 3.2)(B, Sq, M)),
            "c_kv": _w((lambda *s: torch.randn(*s, dtype=torch.bfloat16) * 0.1)(B, Sk, Dc)),
            "w_q": _w((lambda *s: torch.randn(*s, dtype=torch.bfloat16) * 6.4)(M, H, D) * (M ** -0.5)),
            "w_uk": _w((lambda *s: torch.randn(*s, dtype=torch.bfloat16) * 6.4)(Dc, H, D) * (Dc ** -0.5)),
            "w_uv": _w((lambda *s: torch.randn(*s, dtype=torch.bfloat16) * 0.1)(Dc, H, D) * (Dc ** -0.5)),
            "w_o": _w((lambda *s: torch.randn(*s, dtype=torch.bfloat16) * 0.1)(H, D, M) * ((H * D) ** -0.5)),
        }
    _B_in = _B_setup()

    return {
        "A_x": _A_in["x"],
        "A_k_cache": _A_in["k_cache"],
        "A_v_cache": _A_in["v_cache"],
        "A_wq": _A_in["wq"],
        "A_wo": _A_in["wo"],
        "B_x": _B_in["x"],
        "B_c_kv": _B_in["c_kv"],
        "B_w_q": _B_in["w_q"],
        "B_w_uk": _B_in["w_uk"],
        "B_w_uv": _B_in["w_uv"],
        "B_w_o": _B_in["w_o"],
    }

def reference(A_x, A_k_cache, A_v_cache, A_wq, A_wo, B_x, B_c_kv, B_w_q, B_w_uk, B_w_uv, B_w_o):
    def _A_ref(x, k_cache, v_cache, wq, wo):
        B, Sq, D = x.shape
        Hq, Dh = 32, 128
        Hkv, Sk = k_cache.shape[1], k_cache.shape[2]
        W = min(4096, max(256, Sk // 4))
        rep = Hq // Hkv
        q = (lambda x, *d: x.permute(*d))((x @ wq).reshape(B, Sq, Hq, Dh), 0, 2, 1, 3)
        k = (lambda x, n, axis: torch.repeat_interleave(x, n, dim=axis))(k_cache[:, :, Sk - W:, :], rep, axis=1)
        v = (lambda x, n, axis: torch.repeat_interleave(x, n, dim=axis))(v_cache[:, :, Sk - W:, :], rep, axis=1)
        scores = torch.einsum("bhqd,bhkd->bhqk", q, k) * (Dh ** -0.5)
        p = _softmax(scores, axis=-1)
        o = torch.einsum("bhqk,bhkd->bhqd", p, v)
        return (lambda x, *d: x.permute(*d))(o, 0, 2, 1, 3).reshape(B, Sq, Hq * Dh) @ wo

    def _B_ref(x, c_kv, w_q, w_uk, w_uv, w_o):
        B, Sq, M = x.shape[0], x.shape[1], x.shape[2]
        Sk = c_kv.shape[1]
        H, D = w_q.shape[1], w_q.shape[2]
        q = torch.einsum("bqm,mhd->bhqd", x, w_q)
        k = torch.einsum("bkc,chd->bhkd", c_kv, w_uk)
        v = torch.einsum("bkc,chd->bhkd", c_kv, w_uv)
        scores = torch.einsum("bhqd,bhkd->bhqk", q, k) * (D ** -0.5)
        off = Sk - Sq
        qi = torch.arange(Sq)[:, None]
        kj = torch.arange(Sk)[None, :]
        cmask = kj <= (qi + off)
        scores = torch.where(cmask[None, None], scores, -1e9)
        p = _softmax(scores, axis=-1)
        ctx = torch.einsum("bhqk,bhkd->bhqd", p, v)
        return torch.einsum("bhqd,hdm->bqm", ctx, w_o)

    a = _A_ref(A_x, A_k_cache, A_v_cache, A_wq, A_wo)
    b = _B_ref(B_x, B_c_kv, B_w_q, B_w_uk, B_w_uv, B_w_o)
    return torch.concatenate([a, b], axis=-1)
```

```cpp
#include <hip/hip_runtime.h>
#include <hip/hip_bf16.h>
#include <stdint.h>

typedef unsigned short u16;
typedef __attribute__((ext_vector_type(8))) short bf16x8;
typedef __attribute__((ext_vector_type(4))) short s16x4;
typedef __attribute__((ext_vector_type(4))) float f32x4;
typedef __attribute__((ext_vector_type(16))) float f32x16;
typedef __attribute__((ext_vector_type(4))) unsigned u32x4;

#define DI __device__ __forceinline__

constexpr int NB = 2;
constexpr int DM = 512;
constexpr int HQ = 32, HKV = 8, SK = 8192, DH = 128, WIN = 2048;
constexpr int DC = 512;
constexpr float SCALE = 0.088388347648318440f;
constexpr float LOG2E = 1.4426950408889634f;

constexpr int NSA = 8;
constexpr int TA = WIN / NSA;
constexpr int NSB = 64;
constexpr int TB = SK / NSB;

constexpr size_t WS_QA    = 0;
constexpr size_t WS_QABS  = WS_QA + 2 * 4096;
constexpr size_t WS_PMLA  = WS_QABS + 2 * 32 * 512 / 2;
constexpr size_t WS_POA   = WS_PMLA + 2 * 32 * NSA * 2;
constexpr size_t WS_PMLB  = WS_POA + 2 * 32 * NSA * 128;
constexpr size_t WS_POB   = WS_PMLB + 2 * NSB * 32 * 2;
constexpr size_t WS_CTXP  = WS_POB + (size_t)2 * NSB * 32 * 512;
constexpr size_t WS_OUTA  = WS_CTXP + 8 * 2 * 32 * 128;
constexpr size_t WS_OUTB  = WS_OUTA + 128 * 2 * 512;
constexpr size_t WS_END   = WS_OUTB + 128 * 2 * 512;

DI float bf2f(u16 v) { return __uint_as_float(((unsigned)v) << 16); }
DI u16 f2bf(float f) {
  unsigned u = __float_as_uint(f);
  u += 0x7fffu + ((u >> 16) & 1u);
  return (u16)(u >> 16);
}
DI float rbf(float f) { return bf2f(f2bf(f)); }
DI float lo_f(unsigned w) { return __uint_as_float(w << 16); }
DI float hi_f(unsigned w) { return __uint_as_float(w & 0xffff0000u); }

__global__ __launch_bounds__(256) void k1_proj(const u16* __restrict__ xA, const u16* __restrict__ wqA,
                                               const u16* __restrict__ xB, const u16* __restrict__ wqB,
                                               const u16* __restrict__ wuk, float* __restrict__ ws) {
  __shared__ float xs[2][512];
  __shared__ float red[4][2][128];
  __shared__ float qs[2][128];
  const int tid = threadIdx.x, lane = tid & 63, wave = tid >> 6;
  if (blockIdx.x < 256) {
    const int h = blockIdx.x & 31, cc = blockIdx.x >> 5;
    for (int i = tid; i < 1024; i += 256) xs[i >> 9][i & 511] = bf2f(xB[i]);
    __syncthreads();
    const int r = tid >> 4, cq = tid & 15;
    float acc[2][8];
#pragma unroll
    for (int j = 0; j < 8; ++j) { acc[0][j] = 0.f; acc[1][j] = 0.f; }
#pragma unroll
    for (int p = 0; p < 32; p += 8) {
      u32x4 w[8];
#pragma unroll
      for (int u = 0; u < 8; ++u)
        w[u] = *(const u32x4*)(wqB + (size_t)((p + u) * 16 + r) * 4096 + h * 128 + cq * 8);
#pragma unroll
      for (int u = 0; u < 8; ++u) {
        const int m = (p + u) * 16 + r;
        const float x0 = xs[0][m], x1 = xs[1][m];
#pragma unroll
        for (int k = 0; k < 4; ++k) {
          const float a = lo_f(w[u][k]), b = hi_f(w[u][k]);
          acc[0][2 * k] += x0 * a; acc[0][2 * k + 1] += x0 * b;
          acc[1][2 * k] += x1 * a; acc[1][2 * k + 1] += x1 * b;
        }
      }
    }
#pragma unroll
    for (int b = 0; b < 2; ++b)
#pragma unroll
      for (int j = 0; j < 8; ++j) {
        float v = acc[b][j];
        v += __shfl_xor(v, 16); v += __shfl_xor(v, 32);
        acc[b][j] = v;
      }
    if (lane < 16) {
#pragma unroll
      for (int b = 0; b < 2; ++b)
#pragma unroll
        for (int j = 0; j < 8; ++j) red[wave][b][lane * 8 + j] = acc[b][j];
    }
    __syncthreads();
    {
      const int b = tid >> 7, d = tid & 127;
      const float q = red[0][b][d] + red[1][b][d] + red[2][b][d] + red[3][b][d];
      qs[b][d] = rbf(q);
    }
    __syncthreads();
    u16* qabs = (u16*)(ws + WS_QABS);
#pragma unroll
    for (int p = 0; p < 4; ++p) {
      const int c = cc * 64 + p * 16 + r;
      const u32x4 w = *(const u32x4*)(wuk + ((size_t)c * 32 + h) * 128 + cq * 8);
      float p0 = 0.f, p1 = 0.f;
#pragma unroll
      for (int k = 0; k < 4; ++k) {
        const float a = lo_f(w[k]), b = hi_f(w[k]);
        p0 += qs[0][cq * 8 + 2 * k] * a + qs[0][cq * 8 + 2 * k + 1] * b;
        p1 += qs[1][cq * 8 + 2 * k] * a + qs[1][cq * 8 + 2 * k + 1] * b;
      }
#pragma unroll
      for (int o = 1; o < 16; o <<= 1) { p0 += __shfl_xor(p0, o); p1 += __shfl_xor(p1, o); }
      if (cq == 0) {
        qabs[(0 * 32 + h) * 512 + c] = f2bf(p0);
        qabs[(1 * 32 + h) * 512 + c] = f2bf(p1);
      }
    }
  } else {
    const int col0 = (blockIdx.x - 256) * 32;
    for (int i = tid; i < 1024; i += 256) xs[i >> 9][i & 511] = bf2f(xA[i]);
    __syncthreads();
    const int r = tid >> 2, cq = tid & 3;
    float acc[2][8];
#pragma unroll
    for (int j = 0; j < 8; ++j) { acc[0][j] = 0.f; acc[1][j] = 0.f; }
    u32x4 w[8];
#pragma unroll
    for (int u = 0; u < 8; ++u) w[u] = *(const u32x4*)(wqA + (size_t)(u * 64 + r) * 4096 + col0 + cq * 8);
#pragma unroll
    for (int u = 0; u < 8; ++u) {
      const int m = u * 64 + r;
      const float x0 = xs[0][m], x1 = xs[1][m];
#pragma unroll
      for (int k = 0; k < 4; ++k) {
        const float a = lo_f(w[u][k]), b = hi_f(w[u][k]);
        acc[0][2 * k] += x0 * a; acc[0][2 * k + 1] += x0 * b;
        acc[1][2 * k] += x1 * a; acc[1][2 * k + 1] += x1 * b;
      }
    }
#pragma unroll
    for (int b = 0; b < 2; ++b)
#pragma unroll
      for (int j = 0; j < 8; ++j) {
        float v = acc[b][j];
        v += __shfl_xor(v, 4); v += __shfl_xor(v, 8); v += __shfl_xor(v, 16); v += __shfl_xor(v, 32);
        acc[b][j] = v;
      }
    if (lane < 4) {
#pragma unroll
      for (int b = 0; b < 2; ++b)
#pragma unroll
        for (int j = 0; j < 8; ++j) red[wave][b][lane * 8 + j] = acc[b][j];
    }
    __syncthreads();
    if (tid < 64) {
      const int b = tid >> 5, d = tid & 31;
      const float q = red[0][b][d] + red[1][b][d] + red[2][b][d] + red[3][b][d];
      ws[WS_QA + b * 4096 + col0 + d] = rbf(q);
    }
  }
}

constexpr int CS_STRIDE = 520;
constexpr int PS_STRIDE = 136;
constexpr int K2_LDS = 128 * CS_STRIDE * 2 + 32 * PS_STRIDE * 2 + 2 * 4 * 32 * 4;

__global__ __launch_bounds__(256) void k2_attn(const u16* __restrict__ kc, const u16* __restrict__ vc,
                                               const u16* __restrict__ ckv, float* __restrict__ ws) {
  __shared__ __attribute__((aligned(16))) unsigned char smem[K2_LDS];
  const int tid = threadIdx.x, lane = tid & 63, wave = tid >> 6;
  if (blockIdx.x < 128) {
    u16* Cs = (u16*)smem;
    u16* Ps = Cs + 128 * CS_STRIDE;
    float* redm = (float*)(Ps + 32 * PS_STRIDE);
    float* redl = redm + 128;
    const int b = blockIdx.x >> 6, sp = blockIdx.x & 63;
    const int k0 = sp * TB;
    const u16* cbase = ckv + ((size_t)b * SK + k0 + wave * 32) * DC + lane * 8;
    u32x4 tmp[32];
#pragma unroll
    for (int i = 0; i < 32; ++i) tmp[i] = *(const u32x4*)(cbase + (size_t)i * DC);
    const u16* qab = (const u16*)(ws + WS_QABS) + ((size_t)b * 32 + (lane & 31)) * 512 + 8 * (lane >> 5);
    bf16x8 qf[32];
#pragma unroll
    for (int s = 0; s < 32; ++s) qf[s] = *(const bf16x8*)(qab + 16 * s);
#pragma unroll
    for (int i = 0; i < 32; ++i) *(u32x4*)(Cs + (wave * 32 + i) * CS_STRIDE + lane * 8) = tmp[i];
    __syncthreads();
    f32x16 acc;
#pragma unroll
    for (int i = 0; i < 16; ++i) acc[i] = 0.f;
    {
      const u16* arow = Cs + (wave * 32 + (lane & 31)) * CS_STRIDE + 8 * (lane >> 5);
#pragma unroll
      for (int s = 0; s < 32; ++s) {
        const bf16x8 a = *(const bf16x8*)(arow + 16 * s);
        acc = __builtin_amdgcn_mfma_f32_32x32x16_bf16(a, qf[s], acc, 0, 0, 0);
      }
    }
    float mx = -INFINITY;
#pragma unroll
    for (int i = 0; i < 16; ++i) { acc[i] *= SCALE * LOG2E; mx = fmaxf(mx, acc[i]); }
    mx = fmaxf(mx, __shfl_xor(mx, 32));
    if (lane < 32) redm[wave * 32 + lane] = mx;
    __syncthreads();
    const int hd = lane & 31;
    const float m = fmaxf(fmaxf(redm[hd], redm[32 + hd]), fmaxf(redm[64 + hd], redm[96 + hd]));
    float ls = 0.f;
#pragma unroll
    for (int g = 0; g < 4; ++g) {
      s16x4 pk;
#pragma unroll
      for (int j = 0; j < 4; ++j) {
        const float p = __builtin_amdgcn_exp2f(acc[4 * g + j] - m);
        ls += p;
        pk[j] = (short)f2bf(p);
      }
      *(s16x4*)(Ps + hd * PS_STRIDE + wave * 32 + 8 * g + 4 * (lane >> 5)) = pk;
    }
    ls += __shfl_xor(ls, 32);
    if (lane < 32) redl[wave * 32 + lane] = ls;
    __syncthreads();
    if (wave == 0 && lane < 32) {
      float* pml = ws + WS_PMLB + (((size_t)b * NSB + sp) * 32 + lane) * 2;
      pml[0] = m;
      pml[1] = redl[lane] + redl[32 + lane] + redl[64 + lane] + redl[96 + lane];
    }
    f32x16 o[4];
#pragma unroll
    for (int ct = 0; ct < 4; ++ct)
#pragma unroll
      for (int i = 0; i < 16; ++i) o[ct][i] = 0.f;
    const int hh = lane >> 5, blk = (lane >> 4) & 1, q4 = (lane & 15) >> 2, p4 = lane & 3;
#pragma unroll
    for (int s = 0; s < 8; ++s) {
      const bf16x8 a = *(const bf16x8*)(Ps + (lane & 31) * PS_STRIDE + 16 * s + 8 * hh);
#pragma unroll
      for (int ct = 0; ct < 4; ++ct) {
        const int col = wave * 128 + ct * 32 + 16 * blk + 4 * p4;
        const s16x4 lo = __builtin_amdgcn_ds_read_tr16_b64_v4i16(
            (__attribute__((address_space(3))) s16x4*)(Cs + (16 * s + 8 * hh + q4) * CS_STRIDE + col));
        const s16x4 hi = __builtin_amdgcn_ds_read_tr16_b64_v4i16(
            (__attribute__((address_space(3))) s16x4*)(Cs + (16 * s + 8 * hh + 4 + q4) * CS_STRIDE + col));
        const bf16x8 bb = __builtin_shufflevector(lo, hi, 0, 1, 2, 3, 4, 5, 6, 7);
        o[ct] = __builtin_amdgcn_mfma_f32_32x32x16_bf16(a, bb, o[ct], 0, 0, 0);
      }
    }
    float* po = ws + WS_POB + ((size_t)b * NSB + sp) * 32 * 512;
#pragma unroll
    for (int ct = 0; ct < 4; ++ct)
#pragma unroll
      for (int i = 0; i < 16; ++i) {
        const int head = (i & 3) + 8 * (i >> 2) + 4 * hh;
        po[(size_t)head * 512 + wave * 128 + ct * 32 + (lane & 31)] = o[ct][i];
      }
  } else {
    float* mrg = (float*)smem;
    const int id = blockIdx.x - 128;
    const int sp = id & 7, kvh = (id >> 3) & 7, b = id >> 6;
    const int r = lane >> 4, cq = lane & 15;
    const int key0 = (SK - WIN) + sp * TA + wave * 64;
    const size_t base = (((size_t)b * HKV + kvh) * SK + key0 + r) * DH + cq * 8;
    u32x4 kr[16], vr[16];
#pragma unroll
    for (int i = 0; i < 16; ++i) kr[i] = *(const u32x4*)(kc + base + (size_t)i * 4 * DH);
#pragma unroll
    for (int i = 0; i < 16; ++i) vr[i] = *(const u32x4*)(vc + base + (size_t)i * 4 * DH);
    float q[4][8];
#pragma unroll
    for (int g = 0; g < 4; ++g) {
      const float* qp = ws + WS_QA + b * 4096 + (kvh * 4 + g) * 128 + cq * 8;
      const f32x4 a = *(const f32x4*)qp, c = *(const f32x4*)(qp + 4);
      q[g][0] = a[0]; q[g][1] = a[1]; q[g][2] = a[2]; q[g][3] = a[3];
      q[g][4] = c[0]; q[g][5] = c[1]; q[g][6] = c[2]; q[g][7] = c[3];
    }
    float sc[16][4];
#pragma unroll
    for (int i = 0; i < 16; ++i) {
#pragma unroll
      for (int g = 0; g < 4; ++g) {
        float d = 0.f;
#pragma unroll
        for (int k = 0; k < 4; ++k) d += q[g][2 * k] * lo_f(kr[i][k]) + q[g][2 * k + 1] * hi_f(kr[i][k]);
#pragma unroll
        for (int o = 1; o < 16; o <<= 1) d += __shfl_xor(d, o);
        sc[i][g] = d * (SCALE * LOG2E);
      }
    }
    float m[4], l[4], acc[4][8];
#pragma unroll
    for (int g = 0; g < 4; ++g) {
      float mx = sc[0][g];
#pragma unroll
      for (int i = 1; i < 16; ++i) mx = fmaxf(mx, sc[i][g]);
      mx = fmaxf(mx, __shfl_xor(mx, 16)); mx = fmaxf(mx, __shfl_xor(mx, 32));
      m[g] = mx;
      float s = 0.f;
#pragma unroll
      for (int j = 0; j < 8; ++j) acc[g][j] = 0.f;
#pragma unroll
      for (int i = 0; i < 16; ++i) {
        const float p = __builtin_amdgcn_exp2f(sc[i][g] - mx);
        s += p;
#pragma unroll
        for (int k = 0; k < 4; ++k) {
          acc[g][2 * k] += p * lo_f(vr[i][k]);
          acc[g][2 * k + 1] += p * hi_f(vr[i][k]);
        }
      }
      s += __shfl_xor(s, 16); s += __shfl_xor(s, 32);
      l[g] = s;
#pragma unroll
      for (int j = 0; j < 8; ++j) {
        float v = acc[g][j];
        v += __shfl_xor(v, 16); v += __shfl_xor(v, 32);
        acc[g][j] = v;
      }
    }
    if (r == 0) {
#pragma unroll
      for (int g = 0; g < 4; ++g) {
        float* rec = mrg + (wave * 4 + g) * 130;
#pragma unroll
        for (int j = 0; j < 8; ++j) rec[2 + cq * 8 + j] = acc[g][j];
        if (cq == 0) { rec[0] = m[g]; rec[1] = l[g]; }
      }
    }
    __syncthreads();
    {
      const int g = wave, d0 = lane * 2;
      float M = -INFINITY;
#pragma unroll
      for (int w = 0; w < 4; ++w) M = fmaxf(M, mrg[(w * 4 + g) * 130]);
      float L = 0.f, o0 = 0.f, o1 = 0.f;
#pragma unroll
      for (int w = 0; w < 4; ++w) {
        const float* rec = mrg + (w * 4 + g) * 130;
        const float wt = __builtin_amdgcn_exp2f(rec[0] - M);
        L += wt * rec[1]; o0 += wt * rec[2 + d0]; o1 += wt * rec[3 + d0];
      }
      const size_t slot = ((size_t)b * HQ + kvh * 4 + g) * NSA + sp;
      ws[WS_POA + slot * 128 + d0] = o0;
      ws[WS_POA + slot * 128 + d0 + 1] = o1;
      if (lane == 0) { ws[WS_PMLA + slot * 2] = M; ws[WS_PMLA + slot * 2 + 1] = L; }
    }
  }
}

DI void gemv32_partial(const u16* __restrict__ W, int row0, const float (*vec)[32], float (*red)[2][512],
                       float* __restrict__ outp) {
  const int tid = threadIdx.x, lane = tid & 63, wave = tid >> 6;
  float acc[2][8];
#pragma unroll
  for (int j = 0; j < 8; ++j) { acc[0][j] = 0.f; acc[1][j] = 0.f; }
  u32x4 w[8];
#pragma unroll
  for (int u = 0; u < 8; ++u) w[u] = *(const u32x4*)(W + (size_t)(row0 + u * 4 + wave) * 512 + lane * 8);
#pragma unroll
  for (int u = 0; u < 8; ++u) {
    const float x0 = vec[0][u * 4 + wave], x1 = vec[1][u * 4 + wave];
#pragma unroll
    for (int k = 0; k < 4; ++k) {
      const float a = lo_f(w[u][k]), b = hi_f(w[u][k]);
      acc[0][2 * k] += x0 * a; acc[0][2 * k + 1] += x0 * b;
      acc[1][2 * k] += x1 * a; acc[1][2 * k + 1] += x1 * b;
    }
  }
#pragma unroll
  for (int b = 0; b < 2; ++b)
#pragma unroll
    for (int j = 0; j < 8; ++j) red[wave][b][lane * 8 + j] = acc[b][j];
  __syncthreads();
  for (int i = tid; i < 1024; i += 256) {
    const int b = i >> 9, mm = i & 511;
    outp[i] = red[0][b][mm] + red[1][b][mm] + red[2][b][mm] + red[3][b][mm];
  }
}

__global__ __launch_bounds__(256) void k3_comb(const u16* __restrict__ wo, const u16* __restrict__ wuv,
                                               float* __restrict__ ws) {
  __shared__ float red[4][2][512];
  __shared__ float vec[2][32];
  __shared__ float cx[2][64];
  __shared__ float wts[2][64];
  __shared__ float tmp2[2][2][64];
  const int tid = threadIdx.x, lane = tid & 63, wave = tid >> 6;
  if (blockIdx.x < 128) {
    const int j = blockIdx.x, hq = j >> 2, d0 = (j & 3) * 32;
    if (tid < 64) {
      const int b = tid >> 5, d = tid & 31;
      const size_t slot0 = ((size_t)b * HQ + hq) * NSA;
      float M = -INFINITY;
#pragma unroll
      for (int s = 0; s < NSA; ++s) M = fmaxf(M, ws[WS_PMLA + (slot0 + s) * 2]);
      float L = 0.f, o = 0.f;
#pragma unroll
      for (int s = 0; s < NSA; ++s) {
        const float wt = __builtin_amdgcn_exp2f(ws[WS_PMLA + (slot0 + s) * 2] - M);
        L += wt * ws[WS_PMLA + (slot0 + s) * 2 + 1];
        o += wt * ws[WS_POA + (slot0 + s) * 128 + d0 + d];
      }
      vec[b][d] = o / L;
    }
    __syncthreads();
    gemv32_partial(wo, j * 32, vec, red, ws + WS_OUTA + (size_t)j * 1024);
  } else {
    const int id = blockIdx.x - 128, h = id & 31, cc = id >> 5;
    if (tid < 128) {
      const int b = tid >> 6, s = tid & 63;
      const float* pml = ws + WS_PMLB + (((size_t)b * NSB + s) * 32 + h) * 2;
      const float ms = pml[0], lsv = pml[1];
      float M = ms;
#pragma unroll
      for (int o = 1; o < 64; o <<= 1) M = fmaxf(M, __shfl_xor(M, o));
      const float wt = __builtin_amdgcn_exp2f(ms - M);
      float L = wt * lsv;
#pragma unroll
      for (int o = 1; o < 64; o <<= 1) L += __shfl_xor(L, o);
      wts[b][s] = wt / L;
    }
    __syncthreads();
    {
      const int b = tid >> 7, half = (tid >> 6) & 1, c = tid & 63;
      float a = 0.f;
#pragma unroll 8
      for (int s = half * 32; s < half * 32 + 32; ++s)
        a += wts[b][s] * ws[WS_POB + (((size_t)b * NSB + s) * 32 + h) * 512 + cc * 64 + c];
      tmp2[b][half][c] = a;
    }
    __syncthreads();
    if (tid < 128) { const int b = tid >> 6, c = tid & 63; cx[b][c] = tmp2[b][0][c] + tmp2[b][1][c]; }
    __syncthreads();
    const int r = tid >> 4, cq = tid & 15;
    float acc[2][8];
#pragma unroll
    for (int j = 0; j < 8; ++j) { acc[0][j] = 0.f; acc[1][j] = 0.f; }
    u32x4 w[4];
#pragma unroll
    for (int p = 0; p < 4; ++p) w[p] = *(const u32x4*)(wuv + ((size_t)(cc * 64 + p * 16 + r) * 32 + h) * 128 + cq * 8);
#pragma unroll
    for (int p = 0; p < 4; ++p) {
      const float x0 = cx[0][p * 16 + r], x1 = cx[1][p * 16 + r];
#pragma unroll
      for (int k = 0; k < 4; ++k) {
        const float a = lo_f(w[p][k]), b = hi_f(w[p][k]);
        acc[0][2 * k] += x0 * a; acc[0][2 * k + 1] += x0 * b;
        acc[1][2 * k] += x1 * a; acc[1][2 * k + 1] += x1 * b;
      }
    }
#pragma unroll
    for (int b = 0; b < 2; ++b)
#pragma unroll
      for (int j = 0; j < 8; ++j) {
        float v = acc[b][j];
        v += __shfl_xor(v, 16); v += __shfl_xor(v, 32);
        acc[b][j] = v;
      }
    if (lane < 16) {
#pragma unroll
      for (int b = 0; b < 2; ++b)
#pragma unroll
        for (int j = 0; j < 8; ++j) red[wave][b][lane * 8 + j] = acc[b][j];
    }
    __syncthreads();
    {
      const int b = tid >> 7, d = tid & 127;
      ws[WS_CTXP + (((size_t)cc * 2 + b) * 32 + h) * 128 + d] = red[0][b][d] + red[1][b][d] + red[2][b][d] + red[3][b][d];
    }
  }
}

__global__ __launch_bounds__(256) void k4_outb(const u16* __restrict__ wob, float* __restrict__ ws) {
  __shared__ float red[4][2][512];
  __shared__ float vec[2][32];
  const int tid = threadIdx.x;
  const int j = blockIdx.x, h = j >> 2, d0 = (j & 3) * 32;
  if (tid < 64) {
    const int b = tid >> 5, d = tid & 31;
    float a = 0.f;
#pragma unroll
    for (int cc = 0; cc < 8; ++cc) a += ws[WS_CTXP + (((size_t)cc * 2 + b) * 32 + h) * 128 + d0 + d];
    vec[b][d] = a;
  }
  __syncthreads();
  gemv32_partial(wob, j * 32, vec, red, ws + WS_OUTB + (size_t)j * 1024);
}

__global__ __launch_bounds__(256) void k5_final(const float* __restrict__ ws, u16* __restrict__ out) {
  __shared__ float red[4][64];
  const int tid = threadIdx.x, lane = tid & 63, wave = tid >> 6;
  const int o = blockIdx.x * 64 + lane;
  const int part = o >> 10, idx = o & 1023;
  const float* p = ws + (part ? WS_OUTB : WS_OUTA) + idx;
  float a = 0.f;
#pragma unroll 8
  for (int j = wave * 32; j < wave * 32 + 32; ++j) a += p[(size_t)j * 1024];
  red[wave][lane] = a;
  __syncthreads();
  if (wave == 0) {
    const float v = red[0][lane] + red[1][lane] + red[2][lane] + red[3][lane];
    const int b = idx >> 9, m = idx & 511;
    out[b * 1024 + part * 512 + m] = f2bf(v);
  }
}

extern "C" void kernel_launch(void* const* d_in, const int* in_sizes, int n_in,
                              void* d_out, int out_size, void* d_ws, size_t ws_size,
                              hipStream_t stream) {
  const u16* xA  = (const u16*)d_in[0];
  const u16* kc  = (const u16*)d_in[1];
  const u16* vc  = (const u16*)d_in[2];
  const u16* wq  = (const u16*)d_in[3];
  const u16* wo  = (const u16*)d_in[4];
  const u16* xB  = (const u16*)d_in[5];
  const u16* ckv = (const u16*)d_in[6];
  const u16* wqB = (const u16*)d_in[7];
  const u16* wuk = (const u16*)d_in[8];
  const u16* wuv = (const u16*)d_in[9];
  const u16* wob = (const u16*)d_in[10];
  float* ws = (float*)d_ws;
  u16* out = (u16*)d_out;
  k1_proj<<<384, 256, 0, stream>>>(xA, wq, xB, wqB, wuk, ws);
  k2_attn<<<256, 256, 0, stream>>>(kc, vc, ckv, ws);
  k3_comb<<<384, 256, 0, stream>>>(wo, wuv, ws);
  k4_outb<<<128, 256, 0, stream>>>(wob, ws);
  k5_final<<<32, 256, 0, stream>>>(ws, out);
}
```

```cpp
#include <hip/hip_runtime.h>
#include <hip/hip_bf16.h>
#include <stdint.h>

typedef unsigned short u16;
typedef __attribute__((ext_vector_type(8))) short bf16x8;
typedef __attribute__((ext_vector_type(4))) short s16x4;
typedef __attribute__((ext_vector_type(4))) float f32x4;
typedef __attribute__((ext_vector_type(16))) float f32x16;
typedef __attribute__((ext_vector_type(4))) unsigned u32x4;
typedef __attribute__((ext_vector_type(2))) unsigned u32x2;
typedef __attribute__((ext_vector_type(2))) __bf16 bf16x2;

#define DI __device__ __forceinline__

constexpr int HQ = 32, HKV = 8, SK = 8192, DH = 128, WIN = 2048;
constexpr int DC = 512;
constexpr float SCALE = 0.088388347648318440f;
constexpr float LOG2E = 1.4426950408889634f;

constexpr int NSA = 8;
constexpr int TA = WIN / NSA;
constexpr int NSB = 64;
constexpr int TB = SK / NSB;

constexpr size_t WS_QA    = 0;
constexpr size_t WS_QABS  = WS_QA + 4096;
constexpr size_t WS_PMLA  = WS_QABS + 16384;
constexpr size_t WS_POA   = WS_PMLA + 2 * 32 * NSA * 2;
constexpr size_t WS_PMLB  = WS_POA + 2 * 32 * NSA * 128;
constexpr size_t WS_POB   = WS_PMLB + 2 * NSB * 32 * 2;
constexpr size_t WS_OUTA  = WS_POB + (size_t)2 * NSB * 32 * 512 / 2;
constexpr size_t WS_OUTB  = WS_OUTA + 128 * 2 * 512;
constexpr size_t WS_END   = WS_OUTB + 128 * 2 * 512;

DI float bf2f(u16 v) { return __uint_as_float(((unsigned)v) << 16); }
DI u16 f2bf(float f) {
  unsigned u = __float_as_uint(f);
  u += 0x7fffu + ((u >> 16) & 1u);
  return (u16)(u >> 16);
}
DI float rbf(float f) { return bf2f(f2bf(f)); }
DI float lo_f(unsigned w) { return __uint_as_float(w << 16); }
DI float hi_f(unsigned w) { return __uint_as_float(w & 0xffff0000u); }
DI u32x4 ldnt(const u16* p) { return __builtin_nontemporal_load((const u32x4*)p); }
DI u32x4 ldpl(const u16* p) { return *(const u32x4*)p; }
DI float dot2(unsigned a, unsigned b, float acc) {
  return __builtin_amdgcn_fdot2_f32_bf16(__builtin_bit_cast(bf16x2, a), __builtin_bit_cast(bf16x2, b), acc, false);
}
template <int CTRL> DI float dpp(float x) {
  return __builtin_bit_cast(float, __builtin_amdgcn_mov_dpp(__builtin_bit_cast(int, x), CTRL, 0xf, 0xf, true));
}
DI float row16_sum(float v) {
  v += dpp<0xB1>(v); v += dpp<0x4E>(v); v += dpp<0x141>(v); v += dpp<0x140>(v);
  return v;
}
DI float x32_sum(float x) {
  auto s = __builtin_amdgcn_permlane16_swap(__float_as_uint(x), __float_as_uint(x), false, false);
  x = __uint_as_float(s[0]) + __uint_as_float(s[1]);
  auto t = __builtin_amdgcn_permlane32_swap(__float_as_uint(x), __float_as_uint(x), false, false);
  return __uint_as_float(t[0]) + __uint_as_float(t[1]);
}
DI float x32_max(float x) {
  auto s = __builtin_amdgcn_permlane16_swap(__float_as_uint(x), __float_as_uint(x), false, false);
  x = fmaxf(__uint_as_float(s[0]), __uint_as_float(s[1]));
  auto t = __builtin_amdgcn_permlane32_swap(__float_as_uint(x), __float_as_uint(x), false, false);
  return fmaxf(__uint_as_float(t[0]), __uint_as_float(t[1]));
}
DI float h32_sum(float x) {
  auto t = __builtin_amdgcn_permlane32_swap(__float_as_uint(x), __float_as_uint(x), false, false);
  return __uint_as_float(t[0]) + __uint_as_float(t[1]);
}
DI float h32_max(float x) {
  auto t = __builtin_amdgcn_permlane32_swap(__float_as_uint(x), __float_as_uint(x), false, false);
  return fmaxf(__uint_as_float(t[0]), __uint_as_float(t[1]));
}

__global__ __launch_bounds__(256) void k1_proj(const u16* __restrict__ xA, const u16* __restrict__ wqA,
                                               const u16* __restrict__ xB, const u16* __restrict__ wqB,
                                               const u16* __restrict__ wuk, float* __restrict__ ws) {
  __shared__ float xs[2][512];
  __shared__ float red[4][2][128];
  __shared__ float qs[2][128];
  const int tid = threadIdx.x, lane = tid & 63, wave = tid >> 6;
  if (blockIdx.x < 256) {
    const int h = blockIdx.x & 31, cc = blockIdx.x >> 5;
    const int r = tid >> 4, cq = tid & 15;
    u32x4 w[32], wk[4];
#pragma unroll
    for (int u = 0; u < 32; ++u) w[u] = ldpl(wqB + (size_t)(u * 16 + r) * 4096 + h * 128 + cq * 8);
#pragma unroll
    for (int p = 0; p < 4; ++p) wk[p] = ldnt(wuk + ((size_t)(cc * 64 + p * 16 + r) * 32 + h) * 128 + cq * 8);
    {
      const unsigned xv = *(const unsigned*)(xB + tid * 4), xw = *(const unsigned*)(xB + tid * 4 + 2);
      float* xd = &xs[0][0] + tid * 4;
      xd[0] = lo_f(xv); xd[1] = hi_f(xv); xd[2] = lo_f(xw); xd[3] = hi_f(xw);
    }
    __syncthreads();
    float acc[2][8];
#pragma unroll
    for (int j = 0; j < 8; ++j) { acc[0][j] = 0.f; acc[1][j] = 0.f; }
#pragma unroll
    for (int u = 0; u < 32; ++u) {
      const int m = u * 16 + r;
      const float x0 = xs[0][m], x1 = xs[1][m];
#pragma unroll
      for (int k = 0; k < 4; ++k) {
        const float a = lo_f(w[u][k]), b = hi_f(w[u][k]);
        acc[0][2 * k] += x0 * a; acc[0][2 * k + 1] += x0 * b;
        acc[1][2 * k] += x1 * a; acc[1][2 * k + 1] += x1 * b;
      }
    }
#pragma unroll
    for (int b = 0; b < 2; ++b)
#pragma unroll
      for (int j = 0; j < 8; ++j) acc[b][j] = x32_sum(acc[b][j]);
    if (lane < 16) {
#pragma unroll
      for (int b = 0; b < 2; ++b)
#pragma unroll
        for (int j = 0; j < 8; ++j) red[wave][b][lane * 8 + j] = acc[b][j];
    }
    __syncthreads();
    {
      const int b = tid >> 7, d = tid & 127;
      const float q = red[0][b][d] + red[1][b][d] + red[2][b][d] + red[3][b][d];
      qs[b][d] = rbf(q);
    }
    __syncthreads();
    u16* qabs = (u16*)(ws + WS_QABS);
#pragma unroll
    for (int p = 0; p < 4; ++p) {
      const int c = cc * 64 + p * 16 + r;
      float p0 = 0.f, p1 = 0.f;
#pragma unroll
      for (int k = 0; k < 4; ++k) {
        const float a = lo_f(wk[p][k]), b = hi_f(wk[p][k]);
        p0 += qs[0][cq * 8 + 2 * k] * a + qs[0][cq * 8 + 2 * k + 1] * b;
        p1 += qs[1][cq * 8 + 2 * k] * a + qs[1][cq * 8 + 2 * k + 1] * b;
      }
      p0 = row16_sum(p0); p1 = row16_sum(p1);
      if (cq == 0) {
        qabs[(0 * 32 + h) * 512 + c] = f2bf(p0);
        qabs[(1 * 32 + h) * 512 + c] = f2bf(p1);
      }
    }
  } else {
    const int col0 = (blockIdx.x - 256) * 32;
    const int r = tid >> 2, cq = tid & 3;
    u32x4 w[8];
#pragma unroll
    for (int u = 0; u < 8; ++u) w[u] = ldnt(wqA + (size_t)(u * 64 + r) * 4096 + col0 + cq * 8);
    {
      const unsigned xv = *(const unsigned*)(xA + tid * 4), xw = *(const unsigned*)(xA + tid * 4 + 2);
      float* xd = &xs[0][0] + tid * 4;
      xd[0] = lo_f(xv); xd[1] = hi_f(xv); xd[2] = lo_f(xw); xd[3] = hi_f(xw);
    }
    __syncthreads();
    float acc[2][8];
#pragma unroll
    for (int j = 0; j < 8; ++j) { acc[0][j] = 0.f; acc[1][j] = 0.f; }
#pragma unroll
    for (int u = 0; u < 8; ++u) {
      const int m = u * 64 + r;
      const float x0 = xs[0][m], x1 = xs[1][m];
#pragma unroll
      for (int k = 0; k < 4; ++k) {
        const float a = lo_f(w[u][k]), b = hi_f(w[u][k]);
        acc[0][2 * k] += x0 * a; acc[0][2 * k + 1] += x0 * b;
        acc[1][2 * k] += x1 * a; acc[1][2 * k + 1] += x1 * b;
      }
    }
#pragma unroll
    for (int b = 0; b < 2; ++b)
#pragma unroll
      for (int j = 0; j < 8; ++j) {
        float v = acc[b][j];
        v += dpp<0x124>(v); v += dpp<0x128>(v);
        acc[b][j] = x32_sum(v);
      }
    if (lane < 4) {
#pragma unroll
      for (int b = 0; b < 2; ++b)
#pragma unroll
        for (int j = 0; j < 8; ++j) red[wave][b][lane * 8 + j] = acc[b][j];
    }
    __syncthreads();
    if (tid < 64) {
      const int b = tid >> 5, d = tid & 31;
      const float q = red[0][b][d] + red[1][b][d] + red[2][b][d] + red[3][b][d];
      ((u16*)(ws + WS_QA))[b * 4096 + col0 + d] = f2bf(q);
    }
  }
}

constexpr int CS_STRIDE = 520;
constexpr int PS_STRIDE = 136;
constexpr int K2_LDS = 128 * CS_STRIDE * 2 + 32 * PS_STRIDE * 2 + 2 * 4 * 32 * 4;

__global__ __launch_bounds__(256) void k2_attn(const u16* __restrict__ kc, const u16* __restrict__ vc,
                                               const u16* __restrict__ ckv, float* __restrict__ ws) {
  __shared__ __attribute__((aligned(16))) unsigned char smem[K2_LDS];
  const int tid = threadIdx.x, lane = tid & 63, wave = tid >> 6;
  if (blockIdx.x < 128) {
    u16* Cs = (u16*)smem;
    u16* Ps = Cs + 128 * CS_STRIDE;
    float* redm = (float*)(Ps + 32 * PS_STRIDE);
    float* redl = redm + 128;
    const int b = blockIdx.x >> 6, sp = blockIdx.x & 63;
    const int k0 = sp * TB;
    const u16* cbase = ckv + ((size_t)b * SK + k0 + wave * 32) * DC + lane * 8;
    u32x4 tmp[32];
#pragma unroll
    for (int i = 0; i < 32; ++i) tmp[i] = ldnt(cbase + (size_t)i * DC);
    const u16* qab = (const u16*)(ws + WS_QABS) + ((size_t)b * 32 + (lane & 31)) * 512 + 8 * (lane >> 5);
    bf16x8 qf[32];
#pragma unroll
    for (int s = 0; s < 32; ++s) qf[s] = *(const bf16x8*)(qab + 16 * s);
#pragma unroll
    for (int i = 0; i < 32; ++i) *(u32x4*)(Cs + (wave * 32 + i) * CS_STRIDE + lane * 8) = tmp[i];
    __syncthreads();
    f32x16 acc;
#pragma unroll
    for (int i = 0; i < 16; ++i) acc[i] = 0.f;
    {
      const u16* arow = Cs + (wave * 32 + (lane & 31)) * CS_STRIDE + 8 * (lane >> 5);
#pragma unroll
      for (int s = 0; s < 32; ++s) {
        const bf16x8 a = *(const bf16x8*)(arow + 16 * s);
        acc = __builtin_amdgcn_mfma_f32_32x32x16_bf16(a, qf[s], acc, 0, 0, 0);
      }
    }
    float mx = -INFINITY;
#pragma unroll
    for (int i = 0; i < 16; ++i) { acc[i] *= SCALE * LOG2E; mx = fmaxf(mx, acc[i]); }
    mx = h32_max(mx);
    if (lane < 32) redm[wave * 32 + lane] = mx;
    __syncthreads();
    const int hd = lane & 31;
    const float m = fmaxf(fmaxf(redm[hd], redm[32 + hd]), fmaxf(redm[64 + hd], redm[96 + hd]));
    float ls = 0.f;
#pragma unroll
    for (int g = 0; g < 4; ++g) {
      s16x4 pk;
#pragma unroll
      for (int j = 0; j < 4; ++j) {
        const float p = __builtin_amdgcn_exp2f(acc[4 * g + j] - m);
        ls += p;
        pk[j] = (short)f2bf(p);
      }
      *(s16x4*)(Ps + hd * PS_STRIDE + wave * 32 + 8 * g + 4 * (lane >> 5)) = pk;
    }
    ls = h32_sum(ls);
    if (lane < 32) redl[wave * 32 + lane] = ls;
    __syncthreads();
    if (wave == 0 && lane < 32) {
      float* pml = ws + WS_PMLB + (((size_t)b * 32 + lane) * NSB + sp) * 2;
      pml[0] = m;
      pml[1] = redl[lane] + redl[32 + lane] + redl[64 + lane] + redl[96 + lane];
    }
    f32x16 o[4];
#pragma unroll
    for (int ct = 0; ct < 4; ++ct)
#pragma unroll
      for (int i = 0; i < 16; ++i) o[ct][i] = 0.f;
    const int hh = lane >> 5, blk = (lane >> 4) & 1, q4 = (lane & 15) >> 2, p4 = lane & 3;
#pragma unroll
    for (int s = 0; s < 8; ++s) {
      const bf16x8 a = *(const bf16x8*)(Ps + (lane & 31) * PS_STRIDE + 16 * s + 8 * hh);
#pragma unroll
      for (int ct = 0; ct < 4; ++ct) {
        const int col = wave * 128 + ct * 32 + 16 * blk + 4 * p4;
        const s16x4 lo = __builtin_amdgcn_ds_read_tr16_b64_v4i16(
            (__attribute__((address_space(3))) s16x4*)(Cs + (16 * s + 8 * hh + q4) * CS_STRIDE + col));
        const s16x4 hi = __builtin_amdgcn_ds_read_tr16_b64_v4i16(
            (__attribute__((address_space(3))) s16x4*)(Cs + (16 * s + 8 * hh + 4 + q4) * CS_STRIDE + col));
        const bf16x8 bb = __builtin_shufflevector(lo, hi, 0, 1, 2, 3, 4, 5, 6, 7);
        o[ct] = __builtin_amdgcn_mfma_f32_32x32x16_bf16(a, bb, o[ct], 0, 0, 0);
      }
    }
    u16* po = (u16*)(ws + WS_POB);
#pragma unroll
    for (int ct = 0; ct < 4; ++ct)
#pragma unroll
      for (int i = 0; i < 16; ++i) {
        const int head = (i & 3) + 8 * (i >> 2) + 4 * hh;
        po[(((size_t)b * 32 + head) * NSB + sp) * 512 + wave * 128 + ct * 32 + (lane & 31)] = f2bf(o[ct][i]);
      }
  } else {
    float* mrg = (float*)smem;
    const int id = blockIdx.x - 128;
    const int sp = id & 7, kvh = (id >> 3) & 7, b = id >> 6;
    const int r = lane >> 4, cq = lane & 15;
    const int key0 = (SK - WIN) + sp * TA + wave * 64;
    const size_t base = (((size_t)b * HKV + kvh) * SK + key0 + r) * DH + cq * 8;
    u32x4 kr[16], vr[16];
#pragma unroll
    for (int i = 0; i < 16; ++i) kr[i] = ldnt(kc + base + (size_t)i * 4 * DH);
#pragma unroll
    for (int i = 0; i < 16; ++i) vr[i] = ldnt(vc + base + (size_t)i * 4 * DH);
    u32x4 q[4];
#pragma unroll
    for (int g = 0; g < 4; ++g) q[g] = ldpl((const u16*)(ws + WS_QA) + b * 4096 + (kvh * 4 + g) * 128 + cq * 8);
    float sc[16][4];
#pragma unroll
    for (int i = 0; i < 16; ++i) {
#pragma unroll
      for (int g = 0; g < 4; ++g) {
        float d = 0.f;
#pragma unroll
        for (int k = 0; k < 4; ++k) d = dot2(kr[i][k], q[g][k], d);
        sc[i][g] = row16_sum(d) * (SCALE * LOG2E);
      }
    }
    float m[4], l[4], acc[4][8];
#pragma unroll
    for (int g = 0; g < 4; ++g) {
      float mx = sc[0][g];
#pragma unroll
      for (int i = 1; i < 16; ++i) mx = fmaxf(mx, sc[i][g]);
      mx = x32_max(mx);
      m[g] = mx;
      float s = 0.f;
#pragma unroll
      for (int j = 0; j < 8; ++j) acc[g][j] = 0.f;
#pragma unroll
      for (int i = 0; i < 16; ++i) {
        const float p = __builtin_amdgcn_exp2f(sc[i][g] - mx);
        s += p;
#pragma unroll
        for (int k = 0; k < 4; ++k) {
          acc[g][2 * k] += p * lo_f(vr[i][k]);
          acc[g][2 * k + 1] += p * hi_f(vr[i][k]);
        }
      }
      l[g] = x32_sum(s);
#pragma unroll
      for (int j = 0; j < 8; ++j) acc[g][j] = x32_sum(acc[g][j]);
    }
    if (r == 0) {
#pragma unroll
      for (int g = 0; g < 4; ++g) {
        float* rec = mrg + (wave * 4 + g) * 130;
#pragma unroll
        for (int j = 0; j < 8; ++j) rec[2 + cq * 8 + j] = acc[g][j];
        if (cq == 0) { rec[0] = m[g]; rec[1] = l[g]; }
      }
    }
    __syncthreads();
    {
      const int g = wave, d0 = lane * 2;
      float M = -INFINITY;
#pragma unroll
      for (int w = 0; w < 4; ++w) M = fmaxf(M, mrg[(w * 4 + g) * 130]);
      float L = 0.f, o0 = 0.f, o1 = 0.f;
#pragma unroll
      for (int w = 0; w < 4; ++w) {
        const float* rec = mrg + (w * 4 + g) * 130;
        const float wt = __builtin_amdgcn_exp2f(rec[0] - M);
        L += wt * rec[1]; o0 += wt * rec[2 + d0]; o1 += wt * rec[3 + d0];
      }
      const size_t slot = ((size_t)b * HQ + kvh * 4 + g) * NSA + sp;
      *(float2*)(ws + WS_POA + slot * 128 + d0) = make_float2(o0, o1);
      if (lane == 0) { ws[WS_PMLA + slot * 2] = M; ws[WS_PMLA + slot * 2 + 1] = L; }
    }
  }
}

DI void gemv32_tail(const u32x4 (&w)[8], const float (*vec)[32], float (*red)[2][512], float* __restrict__ outp) {
  const int tid = threadIdx.x, lane = tid & 63, wave = tid >> 6;
  float acc[2][8];
#pragma unroll
  for (int j = 0; j < 8; ++j) { acc[0][j] = 0.f; acc[1][j] = 0.f; }
#pragma unroll
  for (int u = 0; u < 8; ++u) {
    const float x0 = vec[0][u * 4 + wave], x1 = vec[1][u * 4 + wave];
#pragma unroll
    for (int k = 0; k < 4; ++k) {
      const float a = lo_f(w[u][k]), b = hi_f(w[u][k]);
      acc[0][2 * k] += x0 * a; acc[0][2 * k + 1] += x0 * b;
      acc[1][2 * k] += x1 * a; acc[1][2 * k + 1] += x1 * b;
    }
  }
#pragma unroll
  for (int b = 0; b < 2; ++b) {
    *(f32x4*)&red[wave][b][lane * 8] = f32x4{acc[b][0], acc[b][1], acc[b][2], acc[b][3]};
    *(f32x4*)&red[wave][b][lane * 8 + 4] = f32x4{acc[b][4], acc[b][5], acc[b][6], acc[b][7]};
  }
  __syncthreads();
  {
    const int i = tid * 4, b = i >> 9, mm = i & 511;
    const f32x4 s = *(const f32x4*)&red[0][b][mm] + *(const f32x4*)&red[1][b][mm] + *(const f32x4*)&red[2][b][mm] + *(const f32x4*)&red[3][b][mm];
    *(f32x4*)(outp + i) = s;
  }
}

__global__ __launch_bounds__(256) void k3_comb(const u16* __restrict__ wo, const u16* __restrict__ wuv,
                                               const u16* __restrict__ wob, float* __restrict__ ws) {
  __shared__ __attribute__((aligned(16))) float red[4][2][512];
  __shared__ float vec[2][32];
  __shared__ float wts[2][64];
  const int tid = threadIdx.x, lane = tid & 63, wave = tid >> 6;
  if (blockIdx.x < 128) {
    const int j = blockIdx.x, hq = j >> 2, d0 = (j & 3) * 32;
    u32x4 w[8];
#pragma unroll
    for (int u = 0; u < 8; ++u) w[u] = ldnt(wo + (size_t)(j * 32 + u * 4 + wave) * 512 + lane * 8);
    if (tid < 64) {
      const int b = tid >> 5, d = tid & 31;
      const size_t slot0 = ((size_t)b * HQ + hq) * NSA;
      float ms[NSA], lv[NSA], ov[NSA];
#pragma unroll
      for (int s = 0; s < NSA; ++s) {
        ms[s] = ws[WS_PMLA + (slot0 + s) * 2]; lv[s] = ws[WS_PMLA + (slot0 + s) * 2 + 1];
        ov[s] = ws[WS_POA + (slot0 + s) * 128 + d0 + d];
      }
      float M = ms[0];
#pragma unroll
      for (int s = 1; s < NSA; ++s) M = fmaxf(M, ms[s]);
      float L = 0.f, o = 0.f;
#pragma unroll
      for (int s = 0; s < NSA; ++s) {
        const float wt = __builtin_amdgcn_exp2f(ms[s] - M);
        L += wt * lv[s]; o += wt * ov[s];
      }
      vec[b][d] = o / L;
    }
    __syncthreads();
    gemv32_tail(w, vec, red, ws + WS_OUTA + (size_t)j * 1024);
  } else {
    const int id = blockIdx.x - 128, h = id & 31, dq = id >> 5;
    u32x4 wv[8], w[8];
    {
      const int r = tid >> 2, cq = tid & 3;
#pragma unroll
      for (int u = 0; u < 8; ++u) wv[u] = ldnt(wuv + ((size_t)(u * 64 + r) * 32 + h) * 128 + dq * 32 + cq * 8);
#pragma unroll
      for (int u = 0; u < 8; ++u) w[u] = ldnt(wob + (size_t)(h * 128 + dq * 32 + u * 4 + wave) * 512 + lane * 8);
    }
    if (tid < 128) {
      const int b = tid >> 6, s = tid & 63;
      const float2 ml = *(const float2*)(ws + WS_PMLB + (((size_t)b * 32 + h) * NSB + s) * 2);
      float M = ml.x;
#pragma unroll
      for (int o = 1; o < 64; o <<= 1) M = fmaxf(M, __shfl_xor(M, o));
      const float wt = __builtin_amdgcn_exp2f(ml.x - M);
      float L = wt * ml.y;
#pragma unroll
      for (int o = 1; o < 64; o <<= 1) L += __shfl_xor(L, o);
      wts[b][s] = wt / L;
    }
    __syncthreads();
    {
      const int b = tid >> 7, half = (tid >> 6) & 1, c8 = tid & 63;
      const u16* pp = (const u16*)(ws + WS_POB) + (((size_t)b * 32 + h) * NSB + half * 32) * 512 + c8 * 8;
      float a[8];
#pragma unroll
      for (int k = 0; k < 8; ++k) a[k] = 0.f;
#pragma unroll
      for (int s0 = 0; s0 < 32; s0 += 16) {
        u32x4 pv[16];
#pragma unroll
        for (int s = 0; s < 16; ++s) pv[s] = ldpl(pp + (size_t)(s0 + s) * 512);
#pragma unroll
        for (int s = 0; s < 16; ++s) {
          const float wt = wts[b][half * 32 + s0 + s];
#pragma unroll
          for (int k = 0; k < 4; ++k) { a[2 * k] += wt * lo_f(pv[s][k]); a[2 * k + 1] += wt * hi_f(pv[s][k]); }
        }
      }
      *(f32x4*)&red[half][b][c8 * 8] = f32x4{a[0], a[1], a[2], a[3]};
      *(f32x4*)&red[half][b][c8 * 8 + 4] = f32x4{a[4], a[5], a[6], a[7]};
    }
    __syncthreads();
    float acc[2][8];
#pragma unroll
    for (int j = 0; j < 8; ++j) { acc[0][j] = 0.f; acc[1][j] = 0.f; }
    {
      const int r = tid >> 2;
#pragma unroll
      for (int u = 0; u < 8; ++u) {
        const int c = u * 64 + r;
        const float x0 = red[0][0][c] + red[1][0][c], x1 = red[0][1][c] + red[1][1][c];
#pragma unroll
        for (int k = 0; k < 4; ++k) {
          const float a = lo_f(wv[u][k]), b = hi_f(wv[u][k]);
          acc[0][2 * k] += x0 * a; acc[0][2 * k + 1] += x0 * b;
          acc[1][2 * k] += x1 * a; acc[1][2 * k + 1] += x1 * b;
        }
      }
    }
#pragma unroll
    for (int b = 0; b < 2; ++b)
#pragma unroll
      for (int j = 0; j < 8; ++j) {
        float v = acc[b][j];
        v += dpp<0x124>(v); v += dpp<0x128>(v);
        acc[b][j] = x32_sum(v);
      }
    __syncthreads();
    if (lane < 4) {
#pragma unroll
      for (int b = 0; b < 2; ++b)
#pragma unroll
        for (int j = 0; j < 8; ++j) red[2 + (wave >> 1)][b][(wave & 1) * 32 + lane * 8 + j] = acc[b][j];
    }
    __syncthreads();
    if (tid < 64) {
      const int b = tid >> 5, d = tid & 31;
      vec[b][d] = red[2][b][d] + red[2][b][32 + d] + red[3][b][d] + red[3][b][32 + d];
    }
    __syncthreads();
    gemv32_tail(w, vec, red, ws + WS_OUTB + (size_t)id * 1024);
  }
}

__global__ __launch_bounds__(256) void k4_final(const float* __restrict__ ws, u16* __restrict__ out) {
  __shared__ float red[4][64];
  const int tid = threadIdx.x, lane = tid & 63, wave = tid >> 6;
  const int o = blockIdx.x * 64 + lane;
  const int part = o >> 10, idx = o & 1023;
  const float* p = ws + (part ? WS_OUTB : WS_OUTA) + idx + (size_t)wave * 32 * 1024;
  float v[32];
#pragma unroll
  for (int j = 0; j < 32; ++j) v[j] = p[(size_t)j * 1024];
  float a = 0.f;
#pragma unroll
  for (int j = 0; j < 32; ++j) a += v[j];
  red[wave][lane] = a;
  __syncthreads();
  if (wave == 0) {
    const float s = red[0][lane] + red[1][lane] + red[2][lane] + red[3][lane];
    const int b = idx >> 9, m = idx & 511;
    out[b * 1024 + part * 512 + m] = f2bf(s);
  }
}

extern "C" void kernel_launch(void* const* d_in, const int* in_sizes, int n_in,
                              void* d_out, int out_size, void* d_ws, size_t ws_size,
                              hipStream_t stream) {
  const u16* xA  = (const u16*)d_in[0];
  const u16* kc  = (const u16*)d_in[1];
  const u16* vc  = (const u16*)d_in[2];
  const u16* wq  = (const u16*)d_in[3];
  const u16* wo  = (const u16*)d_in[4];
  const u16* xB  = (const u16*)d_in[5];
  const u16* ckv = (const u16*)d_in[6];
  const u16* wqB = (const u16*)d_in[7];
  const u16* wuk = (const u16*)d_in[8];
  const u16* wuv = (const u16*)d_in[9];
  const u16* wob = (const u16*)d_in[10];
  float* ws = (float*)d_ws;
  u16* out = (u16*)d_out;
  k1_proj<<<384, 256, 0, stream>>>(xA, wq, xB, wqB, wuk, ws);
  k2_attn<<<256, 256, 0, stream>>>(kc, vc, ckv, ws);
  k3_comb<<<256, 256, 0, stream>>>(wo, wuv, wob, ws);
  k4_final<<<32, 256, 0, stream>>>(ws, out);
}
```

```cpp
#include <hip/hip_runtime.h>
#include <hip/hip_bf16.h>
#include <stdint.h>

typedef unsigned short u16;
typedef __attribute__((ext_vector_type(8))) short bf16x8;
typedef __attribute__((ext_vector_type(4))) short s16x4;
typedef __attribute__((ext_vector_type(4))) float f32x4;
typedef __attribute__((ext_vector_type(16))) float f32x16;
typedef __attribute__((ext_vector_type(4))) unsigned u32x4;
typedef __attribute__((ext_vector_type(2))) unsigned u32x2;
typedef __attribute__((ext_vector_type(2))) __bf16 bf16x2;

#define DI __device__ __forceinline__

constexpr int HQ = 32, HKV = 8, SK = 8192, DH = 128, WIN = 2048;
constexpr int DC = 512;
constexpr float SCALE = 0.088388347648318440f;
constexpr float LOG2E = 1.4426950408889634f;

constexpr int NSA = 8;
constexpr int TA = WIN / NSA;
constexpr int NSB = 64;
constexpr int TB = SK / NSB;

constexpr size_t WS_QA    = 0;
constexpr size_t WS_QABS  = WS_QA + 4096;
constexpr size_t WS_PMLA  = WS_QABS + 16384;
constexpr size_t WS_POA   = WS_PMLA + 2 * 32 * NSA * 2;
constexpr size_t WS_PMLB  = WS_POA + 2 * 32 * NSA * 128;
constexpr size_t WS_POB   = WS_PMLB + 2 * NSB * 32 * 2;
constexpr size_t WS_OUTA  = WS_POB + (size_t)2 * NSB * 32 * 512 / 2;
constexpr size_t WS_OUTB  = WS_OUTA + 128 * 2 * 512;
constexpr size_t WS_CNT   = WS_OUTB + 128 * 2 * 512;
constexpr size_t WS_END   = WS_CNT + 3 * 8 * 32;

DI float bf2f(u16 v) { return __uint_as_float(((unsigned)v) << 16); }
DI u16 f2bf(float f) {
  unsigned u = __float_as_uint(f);
  u += 0x7fffu + ((u >> 16) & 1u);
  return (u16)(u >> 16);
}
DI float rbf(float f) { return bf2f(f2bf(f)); }
DI float lo_f(unsigned w) { return __uint_as_float(w << 16); }
DI float hi_f(unsigned w) { return __uint_as_float(w & 0xffff0000u); }
DI u32x4 ldnt(const u16* p) { return __builtin_nontemporal_load((const u32x4*)p); }
DI u32x4 ldpl(const u16* p) { return *(const u32x4*)p; }
DI float dot2(unsigned a, unsigned b, float acc) {
  return __builtin_amdgcn_fdot2_f32_bf16(__builtin_bit_cast(bf16x2, a), __builtin_bit_cast(bf16x2, b), acc, false);
}
template <int CTRL> DI float dpp(float x) {
  return __builtin_bit_cast(float, __builtin_amdgcn_mov_dpp(__builtin_bit_cast(int, x), CTRL, 0xf, 0xf, true));
}
DI float row16_sum(float v) {
  v += dpp<0xB1>(v); v += dpp<0x4E>(v); v += dpp<0x141>(v); v += dpp<0x140>(v);
  return v;
}
DI float x32_sum(float x) {
  auto s = __builtin_amdgcn_permlane16_swap(__float_as_uint(x), __float_as_uint(x), false, false);
  x = __uint_as_float(s[0]) + __uint_as_float(s[1]);
  auto t = __builtin_amdgcn_permlane32_swap(__float_as_uint(x), __float_as_uint(x), false, false);
  return __uint_as_float(t[0]) + __uint_as_float(t[1]);
}
DI float x32_max(float x) {
  auto s = __builtin_amdgcn_permlane16_swap(__float_as_uint(x), __float_as_uint(x), false, false);
  x = fmaxf(__uint_as_float(s[0]), __uint_as_float(s[1]));
  auto t = __builtin_amdgcn_permlane32_swap(__float_as_uint(x), __float_as_uint(x), false, false);
  return fmaxf(__uint_as_float(t[0]), __uint_as_float(t[1]));
}
DI float h32_sum(float x) {
  auto t = __builtin_amdgcn_permlane32_swap(__float_as_uint(x), __float_as_uint(x), false, false);
  return __uint_as_float(t[0]) + __uint_as_float(t[1]);
}
DI float h32_max(float x) {
  auto t = __builtin_amdgcn_permlane32_swap(__float_as_uint(x), __float_as_uint(x), false, false);
  return fmaxf(__uint_as_float(t[0]), __uint_as_float(t[1]));
}


constexpr int CS_STRIDE = 520;
constexpr int PS_STRIDE = 136;
constexpr int LDS_CS = 0;
constexpr int LDS_PS = 128 * CS_STRIDE * 2;
constexpr int LDS_RD = LDS_PS + 32 * PS_STRIDE * 2;
constexpr int LDS_P1 = LDS_RD + 1024;
constexpr int LDS_TOT = LDS_P1 + 4 * 2 * 128 * 4 + 2 * 128 * 4 + 512;
constexpr unsigned SPIN_MAX = 200000;

typedef __attribute__((address_space(3))) void lds_void;

DI __amdgpu_buffer_rsrc_t mk_rsrc(const void* p, unsigned bytes) {
  return __builtin_amdgcn_make_buffer_rsrc((void*)p, (short)0, (int)bytes, 0x00020000);
}
DI u32x4 ld_sc1(__amdgpu_buffer_rsrc_t r, unsigned off) { return __builtin_amdgcn_raw_buffer_load_b128(r, off, 0, 16); }
DI void st_sc1(__amdgpu_buffer_rsrc_t r, unsigned off, u32x4 v) { __builtin_amdgcn_raw_buffer_store_b128(v, r, off, 0, 16); }
DI float ld_sc1_f(const float* p) { return __hip_atomic_load(p, __ATOMIC_RELAXED, __HIP_MEMORY_SCOPE_AGENT); }
DI void st_sc1_f(float* p, float v) { __hip_atomic_store(p, v, __ATOMIC_RELAXED, __HIP_MEMORY_SCOPE_AGENT); }
DI void st_sc1_u(unsigned* p, unsigned v) { __hip_atomic_store(p, v, __ATOMIC_RELAXED, __HIP_MEMORY_SCOPE_AGENT); }
DI void st_sc1_u2(void* p, u32x2 v) {
  __hip_atomic_store((unsigned long long*)p, __builtin_bit_cast(unsigned long long, v), __ATOMIC_RELAXED, __HIP_MEMORY_SCOPE_AGENT);
}
DI void bar() { asm volatile("s_waitcnt lgkmcnt(0)" ::: "memory"); __builtin_amdgcn_s_barrier(); asm volatile("" ::: "memory"); }

DI void publish(unsigned* cnt) {
  asm volatile("s_waitcnt vmcnt(0)" ::: "memory");
  bar();
  if (threadIdx.x == 0) __hip_atomic_fetch_add(cnt + (blockIdx.x & 7) * 32, 1u, __ATOMIC_RELAXED, __HIP_MEMORY_SCOPE_AGENT);
}
DI void grid_wait(unsigned* cnt, unsigned expect) {
  if (threadIdx.x < 64) {
    const int lane = threadIdx.x;
    for (unsigned it = 0; it < SPIN_MAX; ++it) {
      const unsigned v = lane < 8 ? __hip_atomic_load(cnt + lane * 32, __ATOMIC_RELAXED, __HIP_MEMORY_SCOPE_AGENT) : expect;
      if (__all(v >= expect)) break;
      __builtin_amdgcn_s_sleep(1);
    }
  }
  bar();
}

DI void gemv32_tail(const u32x4 (&w)[8], const float (*vec)[32], float (*red)[2][512], __amdgpu_buffer_rsrc_t rout, unsigned obase) {
  const int tid = threadIdx.x, lane = tid & 63, wave = tid >> 6;
  float acc[2][8];
#pragma unroll
  for (int j = 0; j < 8; ++j) { acc[0][j] = 0.f; acc[1][j] = 0.f; }
#pragma unroll
  for (int u = 0; u < 8; ++u) {
    const float x0 = vec[0][u * 4 + wave], x1 = vec[1][u * 4 + wave];
#pragma unroll
    for (int k = 0; k < 4; ++k) {
      const float a = lo_f(w[u][k]), b = hi_f(w[u][k]);
      acc[0][2 * k] += x0 * a; acc[0][2 * k + 1] += x0 * b;
      acc[1][2 * k] += x1 * a; acc[1][2 * k + 1] += x1 * b;
    }
  }
#pragma unroll
  for (int b = 0; b < 2; ++b) {
    *(f32x4*)&red[wave][b][lane * 8] = f32x4{acc[b][0], acc[b][1], acc[b][2], acc[b][3]};
    *(f32x4*)&red[wave][b][lane * 8 + 4] = f32x4{acc[b][4], acc[b][5], acc[b][6], acc[b][7]};
  }
  bar();
  {
    const int i = tid * 4, b = i >> 9, mm = i & 511;
    const f32x4 s = *(const f32x4*)&red[0][b][mm] + *(const f32x4*)&red[1][b][mm] + *(const f32x4*)&red[2][b][mm] + *(const f32x4*)&red[3][b][mm];
    st_sc1(rout, obase + i * 4, __builtin_bit_cast(u32x4, s));
  }
}

__global__ __launch_bounds__(256) void mega(const u16* __restrict__ xA, const u16* __restrict__ kc, const u16* __restrict__ vc,
                                            const u16* __restrict__ wqA, const u16* __restrict__ wo,
                                            const u16* __restrict__ xB, const u16* __restrict__ ckv,
                                            const u16* __restrict__ wqB, const u16* __restrict__ wuk,
                                            const u16* __restrict__ wuv, const u16* __restrict__ wob,
                                            float* __restrict__ ws, u16* __restrict__ out) {
  __shared__ __attribute__((aligned(16))) unsigned char smem[LDS_TOT];
  const int tid = threadIdx.x, lane = tid & 63, wave = tid >> 6, bid = blockIdx.x;
  const bool isB = bid < 128;
  unsigned* cnt = (unsigned*)(ws + WS_CNT);
  float (*xs)[512] = (float (*)[512])(smem + LDS_PS);
  float (*red1)[2][128] = (float (*)[2][128])(smem + LDS_P1);
  float (*qs)[128] = (float (*)[128])(smem + LDS_P1 + 4 * 2 * 128 * 4);
  u16* qo = (u16*)(smem + LDS_P1 + 4 * 2 * 128 * 4 + 2 * 128 * 4);

  const int h1 = bid & 31, cc = bid >> 5;
  const int r16 = tid >> 4, cq16 = tid & 15, r4 = tid >> 2, cq4 = tid & 3;
  u32x4 w1[32], wk[4], wa[8];
#pragma unroll
  for (int u = 0; u < 32; ++u) w1[u] = ldpl(wqB + (size_t)(u * 16 + r16) * 4096 + h1 * 128 + cq16 * 8);
#pragma unroll
  for (int p = 0; p < 4; ++p) wk[p] = ldnt(wuk + ((size_t)(cc * 64 + p * 16 + r16) * 32 + h1) * 128 + cq16 * 8);
  const unsigned xb0 = *(const unsigned*)(xB + tid * 4), xb1 = *(const unsigned*)(xB + tid * 4 + 2);
  unsigned xa0 = 0, xa1 = 0;
  const int colA = (bid - 128) * 32;
  if (!isB) {
#pragma unroll
    for (int u = 0; u < 8; ++u) wa[u] = ldnt(wqA + (size_t)(u * 64 + r4) * 4096 + colA + cq4 * 8);
    xa0 = *(const unsigned*)(xA + tid * 4); xa1 = *(const unsigned*)(xA + tid * 4 + 2);
  }
  u32x4 kr[16], vr[16];
  const int bB = bid >> 6, spB = bid & 63;
  const int idA = bid - 128, spA = idA & 7, kvh = (idA >> 3) & 7, bA = idA >> 6;
  if (isB) {
    const u16* cbase = ckv + ((size_t)bB * SK + spB * TB + wave * 32) * DC + lane * 8;
#pragma unroll
    for (int i = 0; i < 32; ++i)
      __builtin_amdgcn_global_load_lds((const void*)(cbase + (size_t)i * DC), (lds_void*)(smem + LDS_CS + (wave * 32 + i) * (CS_STRIDE * 2)), 16, 0, 2);
  } else {
    const int key0 = (SK - WIN) + spA * TA + wave * 64;
    const size_t base = (((size_t)bA * HKV + kvh) * SK + key0 + (lane >> 4)) * DH + (lane & 15) * 8;
#pragma unroll
    for (int i = 0; i < 16; ++i) kr[i] = ldnt(kc + base + (size_t)i * 4 * DH);
#pragma unroll
    for (int i = 0; i < 16; ++i) vr[i] = ldnt(vc + base + (size_t)i * 4 * DH);
  }
  u32x4 w3[8], wv[8];
  const int h3 = idA & 31, dq = idA >> 5;
  if (isB) {
#pragma unroll
    for (int u = 0; u < 8; ++u) w3[u] = ldnt(wo + (size_t)(bid * 32 + u * 4 + wave) * 512 + lane * 8);
  } else {
#pragma unroll
    for (int u = 0; u < 8; ++u) wv[u] = ldnt(wuv + ((size_t)(u * 64 + r4) * 32 + h3) * 128 + dq * 32 + cq4 * 8);
#pragma unroll
    for (int u = 0; u < 8; ++u) w3[u] = ldnt(wob + (size_t)(h3 * 128 + dq * 32 + u * 4 + wave) * 512 + lane * 8);
  }

  {
    float* xd = &xs[0][0] + tid * 4;
    xd[0] = lo_f(xb0); xd[1] = hi_f(xb0); xd[2] = lo_f(xb1); xd[3] = hi_f(xb1);
  }
  bar();
  {
    float acc[2][8];
#pragma unroll
    for (int j = 0; j < 8; ++j) { acc[0][j] = 0.f; acc[1][j] = 0.f; }
#pragma unroll
    for (int u = 0; u < 32; ++u) {
      const int m = u * 16 + r16;
      const float x0 = xs[0][m], x1 = xs[1][m];
#pragma unroll
      for (int k = 0; k < 4; ++k) {
        const float a = lo_f(w1[u][k]), b = hi_f(w1[u][k]);
        acc[0][2 * k] += x0 * a; acc[0][2 * k + 1] += x0 * b;
        acc[1][2 * k] += x1 * a; acc[1][2 * k + 1] += x1 * b;
      }
    }
#pragma unroll
    for (int b = 0; b < 2; ++b)
#pragma unroll
      for (int j = 0; j < 8; ++j) acc[b][j] = x32_sum(acc[b][j]);
    if (lane < 16) {
#pragma unroll
      for (int b = 0; b < 2; ++b)
#pragma unroll
        for (int j = 0; j < 8; ++j) red1[wave][b][lane * 8 + j] = acc[b][j];
    }
    bar();
    {
      const int b = tid >> 7, d = tid & 127;
      qs[b][d] = rbf(red1[0][b][d] + red1[1][b][d] + red1[2][b][d] + red1[3][b][d]);
    }
    bar();
#pragma unroll
    for (int p = 0; p < 4; ++p) {
      float p0 = 0.f, p1 = 0.f;
#pragma unroll
      for (int k = 0; k < 4; ++k) {
        const float a = lo_f(wk[p][k]), b = hi_f(wk[p][k]);
        p0 += qs[0][cq16 * 8 + 2 * k] * a + qs[0][cq16 * 8 + 2 * k + 1] * b;
        p1 += qs[1][cq16 * 8 + 2 * k] * a + qs[1][cq16 * 8 + 2 * k + 1] * b;
      }
      p0 = row16_sum(p0); p1 = row16_sum(p1);
      if (cq16 == 0) { qo[p * 16 + r16] = f2bf(p0); qo[64 + p * 16 + r16] = f2bf(p1); }
    }
    bar();
    if (tid < 32) {
      const int b = tid >> 4, i = tid & 15;
      st_sc1_u2((u16*)(ws + WS_QABS) + (b * 32 + h1) * 512 + cc * 64 + i * 4, *(const u32x2*)(qo + b * 64 + i * 4));
    }
  }
  if (!isB) {
    bar();
    {
      float* xd = &xs[0][0] + tid * 4;
      xd[0] = lo_f(xa0); xd[1] = hi_f(xa0); xd[2] = lo_f(xa1); xd[3] = hi_f(xa1);
    }
    bar();
    float acc[2][8];
#pragma unroll
    for (int j = 0; j < 8; ++j) { acc[0][j] = 0.f; acc[1][j] = 0.f; }
#pragma unroll
    for (int u = 0; u < 8; ++u) {
      const int m = u * 64 + r4;
      const float x0 = xs[0][m], x1 = xs[1][m];
#pragma unroll
      for (int k = 0; k < 4; ++k) {
        const float a = lo_f(wa[u][k]), b = hi_f(wa[u][k]);
        acc[0][2 * k] += x0 * a; acc[0][2 * k + 1] += x0 * b;
        acc[1][2 * k] += x1 * a; acc[1][2 * k + 1] += x1 * b;
      }
    }
#pragma unroll
    for (int b = 0; b < 2; ++b)
#pragma unroll
      for (int j = 0; j < 8; ++j) {
        float v = acc[b][j];
        v += dpp<0x124>(v); v += dpp<0x128>(v);
        acc[b][j] = x32_sum(v);
      }
    if (lane < 4) {
#pragma unroll
      for (int b = 0; b < 2; ++b)
#pragma unroll
        for (int j = 0; j < 8; ++j) red1[wave][b][lane * 8 + j] = acc[b][j];
    }
    bar();
    if (tid < 64) {
      const int b = tid >> 5, d = tid & 31;
      qo[tid] = f2bf(red1[0][b][d] + red1[1][b][d] + red1[2][b][d] + red1[3][b][d]);
    }
    bar();
    if (tid < 16) {
      const int b = tid >> 3, i = tid & 7;
      st_sc1_u2((u16*)(ws + WS_QA) + b * 4096 + colA + i * 4, *(const u32x2*)(qo + b * 32 + i * 4));
    }
  }
  publish(cnt);
  grid_wait(cnt, 32);

  if (isB) {
    u16* Cs = (u16*)(smem + LDS_CS);
    u16* Ps = (u16*)(smem + LDS_PS);
    float* redm = (float*)(smem + LDS_RD);
    float* redl = redm + 128;
    const __amdgpu_buffer_rsrc_t rq = mk_rsrc(ws + WS_QABS, 65536);
    bf16x8 qf[32];
#pragma unroll
    for (int s = 0; s < 32; ++s) qf[s] = __builtin_bit_cast(bf16x8, ld_sc1(rq, ((bB * 32 + (lane & 31)) * 512 + 8 * (lane >> 5) + 16 * s) * 2));
    asm volatile("s_waitcnt vmcnt(0)" ::: "memory");
    f32x16 acc;
#pragma unroll
    for (int i = 0; i < 16; ++i) acc[i] = 0.f;
    {
      const u16* arow = Cs + (wave * 32 + (lane & 31)) * CS_STRIDE + 8 * (lane >> 5);
#pragma unroll
      for (int s = 0; s < 32; ++s) {
        const bf16x8 a = *(const bf16x8*)(arow + 16 * s);
        acc = __builtin_amdgcn_mfma_f32_32x32x16_bf16(a, qf[s], acc, 0, 0, 0);
      }
    }
    float mx = -INFINITY;
#pragma unroll
    for (int i = 0; i < 16; ++i) { acc[i] *= SCALE * LOG2E; mx = fmaxf(mx, acc[i]); }
    mx = h32_max(mx);
    if (lane < 32) redm[wave * 32 + lane] = mx;
    bar();
    const int hd = lane & 31;
    const float m = fmaxf(fmaxf(redm[hd], redm[32 + hd]), fmaxf(redm[64 + hd], redm[96 + hd]));
    float ls = 0.f;
#pragma unroll
    for (int g = 0; g < 4; ++g) {
      s16x4 pk;
#pragma unroll
      for (int j = 0; j < 4; ++j) {
        const float p = __builtin_amdgcn_exp2f(acc[4 * g + j] - m);
        ls += p;
        pk[j] = (short)f2bf(p);
      }
      *(s16x4*)(Ps + hd * PS_STRIDE + wave * 32 + 8 * g + 4 * (lane >> 5)) = pk;
    }
    ls = h32_sum(ls);
    if (lane < 32) redl[wave * 32 + lane] = ls;
    bar();
    if (wave == 0 && lane < 32) {
      float* pml = ws + WS_PMLB + (((size_t)bB * 32 + lane) * NSB + spB) * 2;
      st_sc1_f(pml, m);
      st_sc1_f(pml + 1, redl[lane] + redl[32 + lane] + redl[64 + lane] + redl[96 + lane]);
    }
    f32x16 o[4];
#pragma unroll
    for (int ct = 0; ct < 4; ++ct)
#pragma unroll
      for (int i = 0; i < 16; ++i) o[ct][i] = 0.f;
    const int hh = lane >> 5, blk = (lane >> 4) & 1, q4 = (lane & 15) >> 2, p4 = lane & 3;
#pragma unroll
    for (int s = 0; s < 8; ++s) {
      const bf16x8 a = *(const bf16x8*)(Ps + (lane & 31) * PS_STRIDE + 16 * s + 8 * hh);
#pragma unroll
      for (int ct = 0; ct < 4; ++ct) {
        const int col = wave * 128 + ct * 32 + 16 * blk + 4 * p4;
        const s16x4 lo = __builtin_amdgcn_ds_read_tr16_b64_v4i16(
            (__attribute__((address_space(3))) s16x4*)(Cs + (16 * s + 8 * hh + q4) * CS_STRIDE + col));
        const s16x4 hi = __builtin_amdgcn_ds_read_tr16_b64_v4i16(
            (__attribute__((address_space(3))) s16x4*)(Cs + (16 * s + 8 * hh + 4 + q4) * CS_STRIDE + col));
        const bf16x8 bb = __builtin_shufflevector(lo, hi, 0, 1, 2, 3, 4, 5, 6, 7);
        o[ct] = __builtin_amdgcn_mfma_f32_32x32x16_bf16(a, bb, o[ct], 0, 0, 0);
      }
    }
    bar();
    {
      u16* Os = Cs;
#pragma unroll
      for (int ct = 0; ct < 4; ++ct)
#pragma unroll
        for (int i = 0; i < 16; ++i) {
          const int head = (i & 3) + 8 * (i >> 2) + 4 * hh;
          Os[head * 520 + wave * 128 + ct * 32 + (lane & 31)] = f2bf(o[ct][i]);
        }
      bar();
      const __amdgpu_buffer_rsrc_t rp = mk_rsrc(ws + WS_POB, 2 * 32 * NSB * 512 * 2);
#pragma unroll
      for (int u = 0; u < 8; ++u) {
        const int head = u * 4 + wave;
        st_sc1(rp, ((((unsigned)bB * 32 + head) * NSB + spB) * 512 + lane * 8) * 2, *(const u32x4*)(Os + head * 520 + lane * 8));
      }
    }
  } else {
    float* mrg = (float*)(smem + LDS_CS);
    const int r = lane >> 4, cq = lane & 15;
    const __amdgpu_buffer_rsrc_t rq = mk_rsrc(ws + WS_QA, 16384);
    u32x4 q[4];
#pragma unroll
    for (int g = 0; g < 4; ++g) q[g] = ld_sc1(rq, (bA * 4096 + (kvh * 4 + g) * 128 + cq * 8) * 2);
    float sc[16][4];
#pragma unroll
    for (int i = 0; i < 16; ++i) {
#pragma unroll
      for (int g = 0; g < 4; ++g) {
        float d = 0.f;
#pragma unroll
        for (int k = 0; k < 4; ++k) d = dot2(kr[i][k], q[g][k], d);
        sc[i][g] = row16_sum(d) * (SCALE * LOG2E);
      }
    }
    float m[4], l[4], acc[4][8];
#pragma unroll
    for (int g = 0; g < 4; ++g) {
      float mx = sc[0][g];
#pragma unroll
      for (int i = 1; i < 16; ++i) mx = fmaxf(mx, sc[i][g]);
      mx = x32_max(mx);
      m[g] = mx;
      float s = 0.f;
#pragma unroll
      for (int j = 0; j < 8; ++j) acc[g][j] = 0.f;
#pragma unroll
      for (int i = 0; i < 16; ++i) {
        const float p = __builtin_amdgcn_exp2f(sc[i][g] - mx);
        s += p;
#pragma unroll
        for (int k = 0; k < 4; ++k) {
          acc[g][2 * k] += p * lo_f(vr[i][k]);
          acc[g][2 * k + 1] += p * hi_f(vr[i][k]);
        }
      }
      l[g] = x32_sum(s);
#pragma unroll
      for (int j = 0; j < 8; ++j) acc[g][j] = x32_sum(acc[g][j]);
    }
    if (r == 0) {
#pragma unroll
      for (int g = 0; g < 4; ++g) {
        float* rec = mrg + (wave * 4 + g) * 130;
#pragma unroll
        for (int j = 0; j < 8; ++j) rec[2 + cq * 8 + j] = acc[g][j];
        if (cq == 0) { rec[0] = m[g]; rec[1] = l[g]; }
      }
    }
    bar();
    {
      const int g = wave, d0 = lane * 2;
      float M = -INFINITY;
#pragma unroll
      for (int w = 0; w < 4; ++w) M = fmaxf(M, mrg[(w * 4 + g) * 130]);
      float L = 0.f, o0 = 0.f, o1 = 0.f;
#pragma unroll
      for (int w = 0; w < 4; ++w) {
        const float* rec = mrg + (w * 4 + g) * 130;
        const float wt = __builtin_amdgcn_exp2f(rec[0] - M);
        L += wt * rec[1]; o0 += wt * rec[2 + d0]; o1 += wt * rec[3 + d0];
      }
      const size_t slot = ((size_t)bA * HQ + kvh * 4 + g) * NSA + spA;
      st_sc1_u2(ws + WS_POA + slot * 128 + d0, u32x2{__float_as_uint(o0), __float_as_uint(o1)});
      if (lane == 0) st_sc1_u2(ws + WS_PMLA + slot * 2, u32x2{__float_as_uint(M), __float_as_uint(L)});
    }
  }
  publish(cnt + 256);
  grid_wait(cnt + 256, 32);

  float (*red)[2][512] = (float (*)[2][512])(smem + LDS_CS);
  float (*vec)[32] = (float (*)[32])(smem + LDS_PS);
  float (*wts)[64] = (float (*)[64])(smem + LDS_PS + 512);
  const __amdgpu_buffer_rsrc_t rout = mk_rsrc(ws + WS_OUTA, 2 * 128 * 1024 * 4);
  if (isB) {
    const int hq = bid >> 2, d0 = (bid & 3) * 32;
    if (tid < 64) {
      const int b = tid >> 5, d = tid & 31;
      const size_t slot0 = ((size_t)b * HQ + hq) * NSA;
      float ms[NSA], lv[NSA], ov[NSA];
#pragma unroll
      for (int s = 0; s < NSA; ++s) {
        ms[s] = ld_sc1_f(ws + WS_PMLA + (slot0 + s) * 2); lv[s] = ld_sc1_f(ws + WS_PMLA + (slot0 + s) * 2 + 1);
        ov[s] = ld_sc1_f(ws + WS_POA + (slot0 + s) * 128 + d0 + d);
      }
      float M = ms[0];
#pragma unroll
      for (int s = 1; s < NSA; ++s) M = fmaxf(M, ms[s]);
      float L = 0.f, o = 0.f;
#pragma unroll
      for (int s = 0; s < NSA; ++s) {
        const float wt = __builtin_amdgcn_exp2f(ms[s] - M);
        L += wt * lv[s]; o += wt * ov[s];
      }
      vec[b][d] = o / L;
    }
    bar();
    gemv32_tail(w3, vec, red, rout, bid * 4096);
  } else {
    if (tid < 128) {
      const int b = tid >> 6, s = tid & 63;
      const float* pml = ws + WS_PMLB + (((size_t)b * 32 + h3) * NSB + s) * 2;
      const float mv = ld_sc1_f(pml), lv = ld_sc1_f(pml + 1);
      float M = mv;
#pragma unroll
      for (int o = 1; o < 64; o <<= 1) M = fmaxf(M, __shfl_xor(M, o));
      const float wt = __builtin_amdgcn_exp2f(mv - M);
      float L = wt * lv;
#pragma unroll
      for (int o = 1; o < 64; o <<= 1) L += __shfl_xor(L, o);
      wts[b][s] = wt / L;
    }
    bar();
    {
      const int b = tid >> 7, half = (tid >> 6) & 1, c8 = tid & 63;
      const __amdgpu_buffer_rsrc_t rp = mk_rsrc(ws + WS_POB, 2 * 32 * NSB * 512 * 2);
      const unsigned pbase = ((((unsigned)b * 32 + h3) * NSB + half * 32) * 512 + c8 * 8) * 2;
      float a[8];
#pragma unroll
      for (int k = 0; k < 8; ++k) a[k] = 0.f;
#pragma unroll
      for (int s0 = 0; s0 < 32; s0 += 16) {
        u32x4 pv[16];
#pragma unroll
        for (int s = 0; s < 16; ++s) pv[s] = ld_sc1(rp, pbase + (s0 + s) * 1024);
#pragma unroll
        for (int s = 0; s < 16; ++s) {
          const float wt = wts[b][half * 32 + s0 + s];
#pragma unroll
          for (int k = 0; k < 4; ++k) { a[2 * k] += wt * lo_f(pv[s][k]); a[2 * k + 1] += wt * hi_f(pv[s][k]); }
        }
      }
      *(f32x4*)&red[half][b][c8 * 8] = f32x4{a[0], a[1], a[2], a[3]};
      *(f32x4*)&red[half][b][c8 * 8 + 4] = f32x4{a[4], a[5], a[6], a[7]};
    }
    bar();
    float acc[2][8];
#pragma unroll
    for (int j = 0; j < 8; ++j) { acc[0][j] = 0.f; acc[1][j] = 0.f; }
#pragma unroll
    for (int u = 0; u < 8; ++u) {
      const int c = u * 64 + r4;
      const float x0 = red[0][0][c] + red[1][0][c], x1 = red[0][1][c] + red[1][1][c];
#pragma unroll
      for (int k = 0; k < 4; ++k) {
        const float a = lo_f(wv[u][k]), b = hi_f(wv[u][k]);
        acc[0][2 * k] += x0 * a; acc[0][2 * k + 1] += x0 * b;
        acc[1][2 * k] += x1 * a; acc[1][2 * k + 1] += x1 * b;
      }
    }
#pragma unroll
    for (int b = 0; b < 2; ++b)
#pragma unroll
      for (int j = 0; j < 8; ++j) {
        float v = acc[b][j];
        v += dpp<0x124>(v); v += dpp<0x128>(v);
        acc[b][j] = x32_sum(v);
      }
    bar();
    if (lane < 4) {
#pragma unroll
      for (int b = 0; b < 2; ++b)
#pragma unroll
        for (int j = 0; j < 8; ++j) red[2 + (wave >> 1)][b][(wave & 1) * 32 + lane * 8 + j] = acc[b][j];
    }
    bar();
    if (tid < 64) {
      const int b = tid >> 5, d = tid & 31;
      vec[b][d] = red[2][b][d] + red[2][b][32 + d] + red[3][b][d] + red[3][b][32 + d];
    }
    bar();
    gemv32_tail(w3, vec, red, rout, (128 + idA) * 4096);
  }
  publish(cnt + 512);

  if (bid < 32) {
    grid_wait(cnt + 512, 32);
    float* fr = (float*)(smem + LDS_PS);
    const int o = bid * 64 + lane;
    const int part = o >> 10, idx = o & 1023;
    const float* p = ws + WS_OUTA + (size_t)part * 128 * 1024 + idx + (size_t)wave * 32 * 1024;
    float v[32];
#pragma unroll
    for (int j = 0; j < 32; ++j) v[j] = ld_sc1_f(p + (size_t)j * 1024);
    float a = 0.f;
#pragma unroll
    for (int j = 0; j < 32; ++j) a += v[j];
    fr[wave * 64 + lane] = a;
    bar();
    if (wave == 0) {
      const float s = fr[lane] + fr[64 + lane] + fr[128 + lane] + fr[192 + lane];
      const int b = idx >> 9, m = idx & 511;
      out[b * 1024 + part * 512 + m] = f2bf(s);
    }
  }
}

extern "C" void kernel_launch(void* const* d_in, const int* in_sizes, int n_in,
                              void* d_out, int out_size, void* d_ws, size_t ws_size,
                              hipStream_t stream) {
  float* ws = (float*)d_ws;
  hipMemsetAsync(ws + WS_CNT, 0, 3 * 8 * 32 * 4, stream);
  mega<<<256, 256, 0, stream>>>((const u16*)d_in[0], (const u16*)d_in[1], (const u16*)d_in[2], (const u16*)d_in[3],
                                (const u16*)d_in[4], (const u16*)d_in[5], (const u16*)d_in[6], (const u16*)d_in[7],
                                (const u16*)d_in[8], (const u16*)d_in[9], (const u16*)d_in[10], ws, (u16*)d_out);
}
```

```cpp
#include <hip/hip_runtime.h>
#include <hip/hip_bf16.h>
#include <stdint.h>

typedef unsigned short u16;
typedef __attribute__((ext_vector_type(8))) short bf16x8;
typedef __attribute__((ext_vector_type(4))) short s16x4;
typedef __attribute__((ext_vector_type(4))) float f32x4;
typedef __attribute__((ext_vector_type(16))) float f32x16;
typedef __attribute__((ext_vector_type(4))) unsigned u32x4;
typedef __attribute__((ext_vector_type(2))) unsigned u32x2;
typedef __attribute__((ext_vector_type(2))) __bf16 bf16x2;

#define DI __device__ __forceinline__

constexpr int HQ = 32, HKV = 8, SK = 8192, DH = 128, WIN = 2048;
constexpr int DC = 512;
constexpr float SCALE = 0.088388347648318440f;
constexpr float LOG2E = 1.4426950408889634f;

constexpr int NSA = 8;
constexpr int TA = WIN / NSA;
constexpr int NSB = 64;
constexpr int TB = SK / NSB;

constexpr size_t WS_QA    = 0;
constexpr size_t WS_QABS  = WS_QA + 4096;
constexpr size_t WS_PMLA  = WS_QABS + 16384;
constexpr size_t WS_POA   = WS_PMLA + 2 * 32 * NSA * 2;
constexpr size_t WS_PMLB  = WS_POA + 2 * 32 * NSA * 128;
constexpr size_t WS_POB   = WS_PMLB + 2 * NSB * 32 * 2;
constexpr size_t WS_OUTA  = WS_POB + (size_t)2 * NSB * 32 * 512 / 2;
constexpr size_t WS_OUTB  = WS_OUTA + 128 * 2 * 512;
constexpr size_t WS_END   = WS_OUTB + 128 * 2 * 512;

DI float bf2f(u16 v) { return __uint_as_float(((unsigned)v) << 16); }
DI u16 f2bf(float f) {
  unsigned u = __float_as_uint(f);
  u += 0x7fffu + ((u >> 16) & 1u);
  return (u16)(u >> 16);
}
DI float rbf(float f) { return bf2f(f2bf(f)); }
DI float lo_f(unsigned w) { return __uint_as_float(w << 16); }
DI float hi_f(unsigned w) { return __uint_as_float(w & 0xffff0000u); }
DI u32x4 ldnt(const u16* p) { return __builtin_nontemporal_load((const u32x4*)p); }
DI u32x4 ldpl(const u16* p) { return *(const u32x4*)p; }
DI float dot2(unsigned a, unsigned b, float acc) {
  return __builtin_amdgcn_fdot2_f32_bf16(__builtin_bit_cast(bf16x2, a), __builtin_bit_cast(bf16x2, b), acc, false);
}
template <int CTRL> DI float dpp(float x) {
  return __builtin_bit_cast(float, __builtin_amdgcn_mov_dpp(__builtin_bit_cast(int, x), CTRL, 0xf, 0xf, true));
}
DI float row16_sum(float v) {
  v += dpp<0xB1>(v); v += dpp<0x4E>(v); v += dpp<0x141>(v); v += dpp<0x140>(v);
  return v;
}
DI float x32_sum(float x) {
  auto s = __builtin_amdgcn_permlane16_swap(__float_as_uint(x), __float_as_uint(x), false, false);
  x = __uint_as_float(s[0]) + __uint_as_float(s[1]);
  auto t = __builtin_amdgcn_permlane32_swap(__float_as_uint(x), __float_as_uint(x), false, false);
  return __uint_as_float(t[0]) + __uint_as_float(t[1]);
}
DI float x32_max(float x) {
  auto s = __builtin_amdgcn_permlane16_swap(__float_as_uint(x), __float_as_uint(x), false, false);
  x = fmaxf(__uint_as_float(s[0]), __uint_as_float(s[1]));
  auto t = __builtin_amdgcn_permlane32_swap(__float_as_uint(x), __float_as_uint(x), false, false);
  return fmaxf(__uint_as_float(t[0]), __uint_as_float(t[1]));
}
DI float h32_sum(float x) {
  auto t = __builtin_amdgcn_permlane32_swap(__float_as_uint(x), __float_as_uint(x), false, false);
  return __uint_as_float(t[0]) + __uint_as_float(t[1]);
}
DI float h32_max(float x) {
  auto t = __builtin_amdgcn_permlane32_swap(__float_as_uint(x), __float_as_uint(x), false, false);
  return fmaxf(__uint_as_float(t[0]), __uint_as_float(t[1]));
}

__global__ __launch_bounds__(256) void k1_proj(const u16* __restrict__ xA, const u16* __restrict__ wqA,
                                               const u16* __restrict__ xB, const u16* __restrict__ wqB,
                                               const u16* __restrict__ wuk, float* __restrict__ ws) {
  __shared__ float xs[2][512];
  __shared__ float red[4][2][128];
  __shared__ float qs[2][128];
  const int tid = threadIdx.x, lane = tid & 63, wave = tid >> 6;
  if (blockIdx.x < 256) {
    const int h = blockIdx.x & 31, cc = blockIdx.x >> 5;
    const int r = tid >> 4, cq = tid & 15;
    u32x4 w[32], wk[4];
#pragma unroll
    for (int u = 0; u < 32; ++u) w[u] = ldpl(wqB + (size_t)(u * 16 + r) * 4096 + h * 128 + cq * 8);
#pragma unroll
    for (int p = 0; p < 4; ++p) wk[p] = ldnt(wuk + ((size_t)(cc * 64 + p * 16 + r) * 32 + h) * 128 + cq * 8);
    {
      const unsigned xv = *(const unsigned*)(xB + tid * 4), xw = *(const unsigned*)(xB + tid * 4 + 2);
      float* xd = &xs[0][0] + tid * 4;
      xd[0] = lo_f(xv); xd[1] = hi_f(xv); xd[2] = lo_f(xw); xd[3] = hi_f(xw);
    }
    __syncthreads();
    float acc[2][8];
#pragma unroll
    for (int j = 0; j < 8; ++j) { acc[0][j] = 0.f; acc[1][j] = 0.f; }
#pragma unroll
    for (int u = 0; u < 32; ++u) {
      const int m = u * 16 + r;
      const float x0 = xs[0][m], x1 = xs[1][m];
#pragma unroll
      for (int k = 0; k < 4; ++k) {
        const float a = lo_f(w[u][k]), b = hi_f(w[u][k]);
        acc[0][2 * k] += x0 * a; acc[0][2 * k + 1] += x0 * b;
        acc[1][2 * k] += x1 * a; acc[1][2 * k + 1] += x1 * b;
      }
    }
#pragma unroll
    for (int b = 0; b < 2; ++b)
#pragma unroll
      for (int j = 0; j < 8; ++j) acc[b][j] = x32_sum(acc[b][j]);
    if (lane < 16) {
#pragma unroll
      for (int b = 0; b < 2; ++b)
#pragma unroll
        for (int j = 0; j < 8; ++j) red[wave][b][lane * 8 + j] = acc[b][j];
    }
    __syncthreads();
    {
      const int b = tid >> 7, d = tid & 127;
      const float q = red[0][b][d] + red[1][b][d] + red[2][b][d] + red[3][b][d];
      qs[b][d] = rbf(q);
    }
    __syncthreads();
    u16* qabs = (u16*)(ws + WS_QABS);
#pragma unroll
    for (int p = 0; p < 4; ++p) {
      const int c = cc * 64 + p * 16 + r;
      float p0 = 0.f, p1 = 0.f;
#pragma unroll
      for (int k = 0; k < 4; ++k) {
        const float a = lo_f(wk[p][k]), b = hi_f(wk[p][k]);
        p0 += qs[0][cq * 8 + 2 * k] * a + qs[0][cq * 8 + 2 * k + 1] * b;
        p1 += qs[1][cq * 8 + 2 * k] * a + qs[1][cq * 8 + 2 * k + 1] * b;
      }
      p0 = row16_sum(p0); p1 = row16_sum(p1);
      if (cq == 0) {
        qabs[(0 * 32 + h) * 512 + c] = f2bf(p0);
        qabs[(1 * 32 + h) * 512 + c] = f2bf(p1);
      }
    }
  } else {
    const int col0 = (blockIdx.x - 256) * 32;
    const int r = tid >> 2, cq = tid & 3;
    u32x4 w[8];
#pragma unroll
    for (int u = 0; u < 8; ++u) w[u] = ldnt(wqA + (size_t)(u * 64 + r) * 4096 + col0 + cq * 8);
    {
      const unsigned xv = *(const unsigned*)(xA + tid * 4), xw = *(const unsigned*)(xA + tid * 4 + 2);
      float* xd = &xs[0][0] + tid * 4;
      xd[0] = lo_f(xv); xd[1] = hi_f(xv); xd[2] = lo_f(xw); xd[3] = hi_f(xw);
    }
    __syncthreads();
    float acc[2][8];
#pragma unroll
    for (int j = 0; j < 8; ++j) { acc[0][j] = 0.f; acc[1][j] = 0.f; }
#pragma unroll
    for (int u = 0; u < 8; ++u) {
      const int m = u * 64 + r;
      const float x0 = xs[0][m], x1 = xs[1][m];
#pragma unroll
      for (int k = 0; k < 4; ++k) {
        const float a = lo_f(w[u][k]), b = hi_f(w[u][k]);
        acc[0][2 * k] += x0 * a; acc[0][2 * k + 1] += x0 * b;
        acc[1][2 * k] += x1 * a; acc[1][2 * k + 1] += x1 * b;
      }
    }
#pragma unroll
    for (int b = 0; b < 2; ++b)
#pragma unroll
      for (int j = 0; j < 8; ++j) {
        float v = acc[b][j];
        v += dpp<0x124>(v); v += dpp<0x128>(v);
        acc[b][j] = x32_sum(v);
      }
    if (lane < 4) {
#pragma unroll
      for (int b = 0; b < 2; ++b)
#pragma unroll
        for (int j = 0; j < 8; ++j) red[wave][b][lane * 8 + j] = acc[b][j];
    }
    __syncthreads();
    if (tid < 64) {
      const int b = tid >> 5, d = tid & 31;
      const float q = red[0][b][d] + red[1][b][d] + red[2][b][d] + red[3][b][d];
      ((u16*)(ws + WS_QA))[b * 4096 + col0 + d] = f2bf(q);
    }
  }
}

constexpr int CS_STRIDE = 520;
constexpr int PS_STRIDE = 136;
constexpr int K2_LDS = 128 * CS_STRIDE * 2 + 32 * PS_STRIDE * 2 + 2 * 4 * 32 * 4;

__global__ __launch_bounds__(256) void k2_attn(const u16* __restrict__ kc, const u16* __restrict__ vc,
                                               const u16* __restrict__ ckv, float* __restrict__ ws) {
  __shared__ __attribute__((aligned(16))) unsigned char smem[K2_LDS];
  const int tid = threadIdx.x, lane = tid & 63, wave = tid >> 6;
  if (blockIdx.x < 128) {
    u16* Cs = (u16*)smem;
    u16* Ps = Cs + 128 * CS_STRIDE;
    float* redm = (float*)(Ps + 32 * PS_STRIDE);
    float* redl = redm + 128;
    const int b = blockIdx.x >> 6, sp = blockIdx.x & 63;
    const int k0 = sp * TB;
    const u16* cbase = ckv + ((size_t)b * SK + k0 + wave * 32) * DC + lane * 8;
    u32x4 tmp[32];
#pragma unroll
    for (int i = 0; i < 32; ++i) tmp[i] = ldnt(cbase + (size_t)i * DC);
    const u16* qab = (const u16*)(ws + WS_QABS) + ((size_t)b * 32 + (lane & 31)) * 512 + 8 * (lane >> 5);
    bf16x8 qf[32];
#pragma unroll
    for (int s = 0; s < 32; ++s) qf[s] = *(const bf16x8*)(qab + 16 * s);
#pragma unroll
    for (int i = 0; i < 32; ++i) *(u32x4*)(Cs + (wave * 32 + i) * CS_STRIDE + lane * 8) = tmp[i];
    __syncthreads();
    f32x16 acc;
#pragma unroll
    for (int i = 0; i < 16; ++i) acc[i] = 0.f;
    {
      const u16* arow = Cs + (wave * 32 + (lane & 31)) * CS_STRIDE + 8 * (lane >> 5);
#pragma unroll
      for (int s = 0; s < 32; ++s) {
        const bf16x8 a = *(const bf16x8*)(arow + 16 * s);
        acc = __builtin_amdgcn_mfma_f32_32x32x16_bf16(a, qf[s], acc, 0, 0, 0);
      }
    }
    float mx = -INFINITY;
#pragma unroll
    for (int i = 0; i < 16; ++i) { acc[i] *= SCALE * LOG2E; mx = fmaxf(mx, acc[i]); }
    mx = h32_max(mx);
    if (lane < 32) redm[wave * 32 + lane] = mx;
    __syncthreads();
    const int hd = lane & 31;
    const float m = fmaxf(fmaxf(redm[hd], redm[32 + hd]), fmaxf(redm[64 + hd], redm[96 + hd]));
    float ls = 0.f;
#pragma unroll
    for (int g = 0; g < 4; ++g) {
      s16x4 pk;
#pragma unroll
      for (int j = 0; j < 4; ++j) {
        const float p = __builtin_amdgcn_exp2f(acc[4 * g + j] - m);
        ls += p;
        pk[j] = (short)f2bf(p);
      }
      *(s16x4*)(Ps + hd * PS_STRIDE + wave * 32 + 8 * g + 4 * (lane >> 5)) = pk;
    }
    ls = h32_sum(ls);
    if (lane < 32) redl[wave * 32 + lane] = ls;
    __syncthreads();
    if (wave == 0 && lane < 32) {
      float* pml = ws + WS_PMLB + (((size_t)b * 32 + lane) * NSB + sp) * 2;
      pml[0] = m;
      pml[1] = redl[lane] + redl[32 + lane] + redl[64 + lane] + redl[96 + lane];
    }
    f32x16 o[4];
#pragma unroll
    for (int ct = 0; ct < 4; ++ct)
#pragma unroll
      for (int i = 0; i < 16; ++i) o[ct][i] = 0.f;
    const int hh = lane >> 5, blk = (lane >> 4) & 1, q4 = (lane & 15) >> 2, p4 = lane & 3;
#pragma unroll
    for (int s = 0; s < 8; ++s) {
      const bf16x8 a = *(const bf16x8*)(Ps + (lane & 31) * PS_STRIDE + 16 * s + 8 * hh);
#pragma unroll
      for (int ct = 0; ct < 4; ++ct) {
        const int col = wave * 128 + ct * 32 + 16 * blk + 4 * p4;
        const s16x4 lo = __builtin_amdgcn_ds_read_tr16_b64_v4i16(
            (__attribute__((address_space(3))) s16x4*)(Cs + (16 * s + 8 * hh + q4) * CS_STRIDE + col));
        const s16x4 hi = __builtin_amdgcn_ds_read_tr16_b64_v4i16(
            (__attribute__((address_space(3))) s16x4*)(Cs + (16 * s + 8 * hh + 4 + q4) * CS_STRIDE + col));
        const bf16x8 bb = __builtin_shufflevector(lo, hi, 0, 1, 2, 3, 4, 5, 6, 7);
        o[ct] = __builtin_amdgcn_mfma_f32_32x32x16_bf16(a, bb, o[ct], 0, 0, 0);
      }
    }
    u16* po = (u16*)(ws + WS_POB);
#pragma unroll
    for (int ct = 0; ct < 4; ++ct)
#pragma unroll
      for (int i = 0; i < 16; ++i) {
        const int head = (i & 3) + 8 * (i >> 2) + 4 * hh;
        po[(((size_t)b * 32 + head) * NSB + sp) * 512 + wave * 128 + ct * 32 + (lane & 31)] = f2bf(o[ct][i]);
      }
  } else {
    float* mrg = (float*)smem;
    const int id = blockIdx.x - 128;
    const int sp = id & 7, kvh = (id >> 3) & 7, b = id >> 6;
    const int r = lane >> 4, cq = lane & 15;
    const int key0 = (SK - WIN) + sp * TA + wave * 64;
    const size_t base = (((size_t)b * HKV + kvh) * SK + key0 + r) * DH + cq * 8;
    u32x4 kr[16], vr[16];
#pragma unroll
    for (int i = 0; i < 16; ++i) kr[i] = ldnt(kc + base + (size_t)i * 4 * DH);
#pragma unroll
    for (int i = 0; i < 16; ++i) vr[i] = ldnt(vc + base + (size_t)i * 4 * DH);
    u32x4 q[4];
#pragma unroll
    for (int g = 0; g < 4; ++g) q[g] = ldpl((const u16*)(ws + WS_QA) + b * 4096 + (kvh * 4 + g) * 128 + cq * 8);
    float sc[16][4];
#pragma unroll
    for (int i = 0; i < 16; ++i) {
#pragma unroll
      for (int g = 0; g < 4; ++g) {
        float d = 0.f;
#pragma unroll
        for (int k = 0; k < 4; ++k) d = dot2(kr[i][k], q[g][k], d);
        sc[i][g] = row16_sum(d) * (SCALE * LOG2E);
      }
    }
    float m[4], l[4], acc[4][8];
#pragma unroll
    for (int g = 0; g < 4; ++g) {
      float mx = sc[0][g];
#pragma unroll
      for (int i = 1; i < 16; ++i) mx = fmaxf(mx, sc[i][g]);
      mx = x32_max(mx);
      m[g] = mx;
      float s = 0.f;
#pragma unroll
      for (int j = 0; j < 8; ++j) acc[g][j] = 0.f;
#pragma unroll
      for (int i = 0; i < 16; ++i) {
        const float p = __builtin_amdgcn_exp2f(sc[i][g] - mx);
        s += p;
#pragma unroll
        for (int k = 0; k < 4; ++k) {
          acc[g][2 * k] += p * lo_f(vr[i][k]);
          acc[g][2 * k + 1] += p * hi_f(vr[i][k]);
        }
      }
      l[g] = x32_sum(s);
#pragma unroll
      for (int j = 0; j < 8; ++j) acc[g][j] = x32_sum(acc[g][j]);
    }
    if (r == 0) {
#pragma unroll
      for (int g = 0; g < 4; ++g) {
        float* rec = mrg + (wave * 4 + g) * 130;
#pragma unroll
        for (int j = 0; j < 8; ++j) rec[2 + cq * 8 + j] = acc[g][j];
        if (cq == 0) { rec[0] = m[g]; rec[1] = l[g]; }
      }
    }
    __syncthreads();
    {
      const int g = wave, d0 = lane * 2;
      float M = -INFINITY;
#pragma unroll
      for (int w = 0; w < 4; ++w) M = fmaxf(M, mrg[(w * 4 + g) * 130]);
      float L = 0.f, o0 = 0.f, o1 = 0.f;
#pragma unroll
      for (int w = 0; w < 4; ++w) {
        const float* rec = mrg + (w * 4 + g) * 130;
        const float wt = __builtin_amdgcn_exp2f(rec[0] - M);
        L += wt * rec[1]; o0 += wt * rec[2 + d0]; o1 += wt * rec[3 + d0];
      }
      const size_t slot = ((size_t)b * HQ + kvh * 4 + g) * NSA + sp;
      *(float2*)(ws + WS_POA + slot * 128 + d0) = make_float2(o0, o1);
      if (lane == 0) { ws[WS_PMLA + slot * 2] = M; ws[WS_PMLA + slot * 2 + 1] = L; }
    }
  }
}

DI void gemv32_tail(const u32x4 (&w)[8], const float (*vec)[32], float (*red)[2][512], float* __restrict__ outp) {
  const int tid = threadIdx.x, lane = tid & 63, wave = tid >> 6;
  float acc[2][8];
#pragma unroll
  for (int j = 0; j < 8; ++j) { acc[0][j] = 0.f; acc[1][j] = 0.f; }
#pragma unroll
  for (int u = 0; u < 8; ++u) {
    const float x0 = vec[0][u * 4 + wave], x1 = vec[1][u * 4 + wave];
#pragma unroll
    for (int k = 0; k < 4; ++k) {
      const float a = lo_f(w[u][k]), b = hi_f(w[u][k]);
      acc[0][2 * k] += x0 * a; acc[0][2 * k + 1] += x0 * b;
      acc[1][2 * k] += x1 * a; acc[1][2 * k + 1] += x1 * b;
    }
  }
#pragma unroll
  for (int b = 0; b < 2; ++b) {
    *(f32x4*)&red[wave][b][lane * 8] = f32x4{acc[b][0], acc[b][1], acc[b][2], acc[b][3]};
    *(f32x4*)&red[wave][b][lane * 8 + 4] = f32x4{acc[b][4], acc[b][5], acc[b][6], acc[b][7]};
  }
  __syncthreads();
  {
    const int i = tid * 4, b = i >> 9, mm = i & 511;
    const f32x4 s = *(const f32x4*)&red[0][b][mm] + *(const f32x4*)&red[1][b][mm] + *(const f32x4*)&red[2][b][mm] + *(const f32x4*)&red[3][b][mm];
    *(f32x4*)(outp + i) = s;
  }
}

__global__ __launch_bounds__(256) void k3_comb(const u16* __restrict__ wo, const u16* __restrict__ wuv,
                                               const u16* __restrict__ wob, float* __restrict__ ws) {
  __shared__ __attribute__((aligned(16))) float red[4][2][512];
  __shared__ float vec[2][32];
  __shared__ float wts[2][64];
  const int tid = threadIdx.x, lane = tid & 63, wave = tid >> 6;
  if (blockIdx.x < 128) {
    const int j = blockIdx.x, hq = j >> 2, d0 = (j & 3) * 32;
    float ms[NSA], lv[NSA], ov[NSA];
    {
      const int b = (tid >> 5) & 1, d = tid & 31;
      const size_t slot0 = ((size_t)b * HQ + hq) * NSA;
#pragma unroll
      for (int s = 0; s < NSA; ++s) {
        ms[s] = ws[WS_PMLA + (slot0 + s) * 2]; lv[s] = ws[WS_PMLA + (slot0 + s) * 2 + 1];
        ov[s] = ws[WS_POA + (slot0 + s) * 128 + d0 + d];
      }
    }
    u32x4 w[8];
#pragma unroll
    for (int u = 0; u < 8; ++u) w[u] = ldnt(wo + (size_t)(j * 32 + u * 4 + wave) * 512 + lane * 8);
    if (tid < 64) {
      const int b = tid >> 5, d = tid & 31;
      float M = ms[0];
#pragma unroll
      for (int s = 1; s < NSA; ++s) M = fmaxf(M, ms[s]);
      float L = 0.f, o = 0.f;
#pragma unroll
      for (int s = 0; s < NSA; ++s) {
        const float wt = __builtin_amdgcn_exp2f(ms[s] - M);
        L += wt * lv[s]; o += wt * ov[s];
      }
      vec[b][d] = o / L;
    }
    __syncthreads();
    gemv32_tail(w, vec, red, ws + WS_OUTA + (size_t)j * 1024);
  } else {
    const int id = blockIdx.x - 128, h = id & 31, dq = id >> 5;
    const float2 ml = *(const float2*)(ws + WS_PMLB + (((size_t)((tid >> 6) & 1) * 32 + h) * NSB + (tid & 63)) * 2);
    u32x4 pvv[32];
    {
      const int b = tid >> 7, half = (tid >> 6) & 1, c8 = tid & 63;
      const u16* pp = (const u16*)(ws + WS_POB) + (((size_t)b * 32 + h) * NSB + half * 32) * 512 + c8 * 8;
#pragma unroll
      for (int s = 0; s < 32; ++s) pvv[s] = ldpl(pp + (size_t)s * 512);
    }
    u32x4 wv[8], w[8];
    {
      const int r = tid >> 2, cq = tid & 3;
#pragma unroll
      for (int u = 0; u < 8; ++u) wv[u] = ldnt(wuv + ((size_t)(u * 64 + r) * 32 + h) * 128 + dq * 32 + cq * 8);
#pragma unroll
      for (int u = 0; u < 8; ++u) w[u] = ldnt(wob + (size_t)(h * 128 + dq * 32 + u * 4 + wave) * 512 + lane * 8);
    }
    if (tid < 128) {
      const int b = tid >> 6, s = tid & 63;
      float M = ml.x;
#pragma unroll
      for (int o = 1; o < 64; o <<= 1) M = fmaxf(M, __shfl_xor(M, o));
      const float wt = __builtin_amdgcn_exp2f(ml.x - M);
      float L = wt * ml.y;
#pragma unroll
      for (int o = 1; o < 64; o <<= 1) L += __shfl_xor(L, o);
      wts[b][s] = wt / L;
    }
    __syncthreads();
    {
      const int b = tid >> 7, half = (tid >> 6) & 1, c8 = tid & 63;
      float a[8];
#pragma unroll
      for (int k = 0; k < 8; ++k) a[k] = 0.f;
#pragma unroll
      for (int s = 0; s < 32; ++s) {
        const float wt = wts[b][half * 32 + s];
#pragma unroll
        for (int k = 0; k < 4; ++k) { a[2 * k] += wt * lo_f(pvv[s][k]); a[2 * k + 1] += wt * hi_f(pvv[s][k]); }
      }
      *(f32x4*)&red[half][b][c8 * 8] = f32x4{a[0], a[1], a[2], a[3]};
      *(f32x4*)&red[half][b][c8 * 8 + 4] = f32x4{a[4], a[5], a[6], a[7]};
    }
    __syncthreads();
    float acc[2][8];
#pragma unroll
    for (int j = 0; j < 8; ++j) { acc[0][j] = 0.f; acc[1][j] = 0.f; }
    {
      const int r = tid >> 2;
#pragma unroll
      for (int u = 0; u < 8; ++u) {
        const int c = u * 64 + r;
        const float x0 = red[0][0][c] + red[1][0][c], x1 = red[0][1][c] + red[1][1][c];
#pragma unroll
        for (int k = 0; k < 4; ++k) {
          const float a = lo_f(wv[u][k]), b = hi_f(wv[u][k]);
          acc[0][2 * k] += x0 * a; acc[0][2 * k + 1] += x0 * b;
          acc[1][2 * k] += x1 * a; acc[1][2 * k + 1] += x1 * b;
        }
      }
    }
#pragma unroll
    for (int b = 0; b < 2; ++b)
#pragma unroll
      for (int j = 0; j < 8; ++j) {
        float v = acc[b][j];
        v += dpp<0x124>(v); v += dpp<0x128>(v);
        acc[b][j] = x32_sum(v);
      }
    __syncthreads();
    if (lane < 4) {
#pragma unroll
      for (int b = 0; b < 2; ++b)
#pragma unroll
        for (int j = 0; j < 8; ++j) red[2 + (wave >> 1)][b][(wave & 1) * 32 + lane * 8 + j] = acc[b][j];
    }
    __syncthreads();
    if (tid < 64) {
      const int b = tid >> 5, d = tid & 31;
      vec[b][d] = red[2][b][d] + red[2][b][32 + d] + red[3][b][d] + red[3][b][32 + d];
    }
    __syncthreads();
    gemv32_tail(w, vec, red, ws + WS_OUTB + (size_t)id * 1024);
  }
}

__global__ __launch_bounds__(256) void k4_final(const float* __restrict__ ws, u16* __restrict__ out) {
  __shared__ float red[4][64];
  const int tid = threadIdx.x, lane = tid & 63, wave = tid >> 6;
  const int o = blockIdx.x * 64 + lane;
  const int part = o >> 10, idx = o & 1023;
  const float* p = ws + (part ? WS_OUTB : WS_OUTA) + idx + (size_t)wave * 32 * 1024;
  float v[32];
#pragma unroll
  for (int j = 0; j < 32; ++j) v[j] = p[(size_t)j * 1024];
  float a = 0.f;
#pragma unroll
  for (int j = 0; j < 32; ++j) a += v[j];
  red[wave][lane] = a;
  __syncthreads();
  if (wave == 0) {
    const float s = red[0][lane] + red[1][lane] + red[2][lane] + red[3][lane];
    const int b = idx >> 9, m = idx & 511;
    out[b * 1024 + part * 512 + m] = f2bf(s);
  }
}

extern "C" void kernel_launch(void* const* d_in, const int* in_sizes, int n_in,
                              void* d_out, int out_size, void* d_ws, size_t ws_size,
                              hipStream_t stream) {
  const u16* xA  = (const u16*)d_in[0];
  const u16* kc  = (const u16*)d_in[1];
  const u16* vc  = (const u16*)d_in[2];
  const u16* wq  = (const u16*)d_in[3];
  const u16* wo  = (const u16*)d_in[4];
  const u16* xB  = (const u16*)d_in[5];
  const u16* ckv = (const u16*)d_in[6];
  const u16* wqB = (const u16*)d_in[7];
  const u16* wuk = (const u16*)d_in[8];
  const u16* wuv = (const u16*)d_in[9];
  const u16* wob = (const u16*)d_in[10];
  float* ws = (float*)d_ws;
  u16* out = (u16*)d_out;
  k1_proj<<<384, 256, 0, stream>>>(xA, wq, xB, wqB, wuk, ws);
  k2_attn<<<256, 256, 0, stream>>>(kc, vc, ckv, ws);
  k3_comb<<<256, 256, 0, stream>>>(wo, wuv, wob, ws);
  k4_final<<<32, 256, 0, stream>>>(ws, out);
}
```
